```python
import torch

def _softmax(x, axis=-1):
    return torch.softmax(x, dim=axis)
def _gelu(x):
    return torch.nn.functional.gelu(x, approximate="tanh")
def _silu(x):
    return torch.nn.functional.silu(x)
def _sigmoid(x):
    return torch.sigmoid(x)
def _layernorm(x, axis=-1, eps=1e-5):
    m = x.mean(dim=axis, keepdim=True)
    v = x.var(dim=axis, keepdim=True, unbiased=False)
    return (x - m) / torch.sqrt(v + eps)
def _scatter_add(tgt, idx, src, axis=0):
    return tgt.clone().index_add_(axis, idx, src)
def _segment_sum(src, seg, n):
    out = torch.zeros((n,) + src.shape[1:], dtype=src.dtype)
    return out.index_add_(0, seg, src)
def _pad(x, pw):
    flat = tuple(v for pair in reversed(pw) for v in pair)
    return torch.nn.functional.pad(x, flat)

kernel_name = 'parallel_sum__mha_block__glu_gated_swiglu__prompt'

def setup_inputs():

    _w = (lambda a: (lambda t: t.to(torch.bfloat16) if t.is_floating_point() else t)(torch.as_tensor(a)))
    def _A_setup():
        B, S, D, H = 1, 2048, 576, 32
        return {
            "x": _w((lambda *s: torch.randn(*s, dtype=torch.bfloat16) * 0.05)(B, S, D)),
            "wqkv": _w((lambda *s: torch.randn(*s, dtype=torch.bfloat16) * 0.05)(D, 3 * D)),
            "wo": _w((lambda *s: torch.randn(*s, dtype=torch.bfloat16) * 0.05)(D, D)),
        }
    _A_in = _A_setup()

    def _B_setup():
        B, S, D, F = 1, 2048, 576, 4608
        return {
            "x": _w((lambda *s: torch.randn(*s, dtype=torch.bfloat16) * 0.05)(B, S, D)),
            "w1": _w((lambda *s: torch.randn(*s, dtype=torch.bfloat16) * 0.4)(D, F) * (2.0 / D) ** 0.5),
            "w3": _w((lambda *s: torch.randn(*s, dtype=torch.bfloat16) * 0.4)(D, F) * (2.0 / D) ** 0.5),
            "w2": _w((lambda *s: torch.randn(*s, dtype=torch.bfloat16) * 0.4)(F, D) * (2.0 / F) ** 0.5),
        }
    _B_in = _B_setup()

    return {
        "A_x": _A_in["x"],
        "A_wqkv": _A_in["wqkv"],
        "A_wo": _A_in["wo"],
        "B_x": _B_in["x"],
        "B_w1": _B_in["w1"],
        "B_w3": _B_in["w3"],
        "B_w2": _B_in["w2"],
    }

def reference(A_x, A_wqkv, A_wo, B_x, B_w1, B_w3, B_w2):
    def _A_ref(x, wqkv, wo):
        B, S, D = x.shape
        H = 32
        Dh = D // H
        qkv = (lambda x, *d: x.permute(*d))((x @ wqkv).reshape(B, S, 3, H, Dh), 2, 0, 3, 1, 4)
        q, k, v = qkv[0], qkv[1], qkv[2]
        scores = torch.einsum("bhqd,bhkd->bhqk", q, k) * (Dh ** -0.5)
        p = _softmax(scores, axis=-1)
        o = torch.einsum("bhqk,bhkd->bhqd", p, v)
        return (lambda x, *d: x.permute(*d))(o, 0, 2, 1, 3).reshape(B, S, D) @ wo

    def _B_ref(x, w1, w3, w2):
        gate = torch.einsum("bsd,df->bsf", x, w1)
        up = torch.einsum("bsd,df->bsf", x, w3)
        h = _silu(gate) * up
        out = torch.einsum("bsf,fd->bsd", h, w2)
        return out

    x = A_x
    xn = _layernorm(x, axis=-1)
    return x + _A_ref(xn, A_wqkv, A_wo) + _B_ref(xn, B_w1, B_w3, B_w2)
```

```cpp
#include <hip/hip_runtime.h>
#include <hip/hip_bf16.h>
#include <stdint.h>

typedef unsigned short u16;
typedef short bf16x8 __attribute__((ext_vector_type(8)));
typedef short s16x4 __attribute__((ext_vector_type(4)));
typedef float f32x16 __attribute__((ext_vector_type(16)));
typedef float f32x4 __attribute__((ext_vector_type(4)));
typedef float f32x2 __attribute__((ext_vector_type(2)));
typedef unsigned u32x4 __attribute__((ext_vector_type(4)));
typedef unsigned u32x2 __attribute__((ext_vector_type(2)));
typedef __bf16 bf16x2_t __attribute__((ext_vector_type(2)));
typedef short v4i16_t __attribute__((ext_vector_type(4)));
typedef __attribute__((address_space(3))) const char* lds_cptr;

#define DI __device__ __forceinline__
#define MFMA(a, b, c) __builtin_amdgcn_mfma_f32_32x32x16_bf16((a), (b), (c), 0, 0, 0)

constexpr int S_ = 2048, D_ = 576, H_ = 32, DH_ = 18, F_ = 4608;
constexpr size_t WS_XN = 0;
constexpr size_t WS_Q0 = WS_XN + (size_t)S_ * D_ * 2;
constexpr size_t WS_Q1 = WS_Q0 + (size_t)H_ * 65536;
constexpr size_t WS_KV = WS_Q1 + (size_t)H_ * 8192;
constexpr size_t KV_HEAD = 155648, KV_K1 = 65536, KV_V = 73728;
constexpr size_t WS_H = WS_KV + (size_t)H_ * KV_HEAD;
constexpr size_t WS_O = WS_H + (size_t)S_ * F_ * 2;
constexpr size_t WS_P = WS_O + (size_t)S_ * D_ * 2;
constexpr size_t WS_END = WS_P + (size_t)8 * S_ * D_ * 4;

DI unsigned pack2(float a, float b) {
  bf16x2_t r = __builtin_convertvector((f32x2){a, b}, bf16x2_t);
  return __builtin_bit_cast(unsigned, r);
}
DI u16 f2bf(float a) { return (u16)(pack2(a, a) & 0xffffu); }
DI float bflo(unsigned w) { return __uint_as_float(w << 16); }
DI float bfhi(unsigned w) { return __uint_as_float(w & 0xffff0000u); }
DI int crow(int r, int hi) { return (r & 3) + 8 * (r >> 2) + 4 * hi; }

DI void glds16(const void* g, unsigned lds_base) {
  unsigned sv;
  asm volatile("s_mov_b32 %0, m0\n\ts_mov_b32 m0, %2\n\ts_nop 0\n\tglobal_load_lds_dwordx4 %1, off\n\ts_mov_b32 m0, %0"
               : "=&s"(sv) : "v"(g), "s"(lds_base) : "memory");
}
DI s16x4 vtr(lds_cptr p) {
  return __builtin_bit_cast(s16x4, __builtin_amdgcn_ds_read_tr16_b64_v4i16((__attribute__((address_space(3))) v4i16_t*)p));
}
DI bf16x8 wfrag(lds_cptr p) {
  s16x4 lo = vtr(p), hi = vtr(p + 256);
  return (bf16x8){lo[0], lo[1], lo[2], lo[3], hi[0], hi[1], hi[2], hi[3]};
}
#define WAIT_BAR(N) asm volatile("s_waitcnt vmcnt(" #N ") lgkmcnt(0)\n\ts_barrier" ::: "memory")

__global__ __launch_bounds__(256) void k_ln(const u16* __restrict__ x, char* __restrict__ ws) {
  const int lane = threadIdx.x & 63, wid = threadIdx.x >> 6;
  const int row = blockIdx.x * 4 + wid;
  const u16* xr = x + (size_t)row * D_;
  u32x4 a = *(const u32x4*)(xr + lane * 8);
  u32x4 b = {0, 0, 0, 0};
  if (lane < 8) b = *(const u32x4*)(xr + 512 + lane * 8);
  float v[16];
#pragma unroll
  for (int i = 0; i < 4; ++i) { v[2 * i] = bflo(a[i]); v[2 * i + 1] = bfhi(a[i]); v[8 + 2 * i] = bflo(b[i]); v[9 + 2 * i] = bfhi(b[i]); }
  float s = 0.f;
#pragma unroll
  for (int i = 0; i < 16; ++i) s += v[i];
#pragma unroll
  for (int o = 32; o >= 1; o >>= 1) s += __shfl_xor(s, o);
  const float mean = s * (1.0f / D_);
  float q = 0.f;
#pragma unroll
  for (int i = 0; i < 8; ++i) { float d = v[i] - mean; q += d * d; }
  if (lane < 8) {
#pragma unroll
    for (int i = 8; i < 16; ++i) { float d = v[i] - mean; q += d * d; }
  }
#pragma unroll
  for (int o = 32; o >= 1; o >>= 1) q += __shfl_xor(q, o);
  const float rstd = 1.0f / sqrtf(q * (1.0f / D_) + 1e-5f);
  u16* xn = (u16*)(ws + WS_XN) + (size_t)row * D_;
  u32x4 oa, ob;
#pragma unroll
  for (int i = 0; i < 4; ++i) {
    oa[i] = pack2((v[2 * i] - mean) * rstd, (v[2 * i + 1] - mean) * rstd);
    ob[i] = pack2((v[8 + 2 * i] - mean) * rstd, (v[9 + 2 * i] - mean) * rstd);
  }
  *(u32x4*)(xn + lane * 8) = oa;
  if (lane < 8) *(u32x4*)(xn + 512 + lane * 8) = ob;
  if (threadIdx.x < 32) {
    const int g = blockIdx.x * 16 + (threadIdx.x >> 1), which = threadIdx.x & 1;
    const int head = g >> 8, gr = g & 255;
    const unsigned w = which ? 0u : 0x3f803f80u;
    *(u32x4*)(ws + WS_KV + (size_t)head * KV_HEAD + KV_V + ((size_t)gr * 20 + 18 + which) * 16) = (u32x4){w, w, w, w};
  }
}

constexpr int NSLOT = 4, SLOT_B = 24576;
constexpr int GEMM_LDS = NSLOT * SLOT_B;

struct UnitDesc { const u16* b0; const u16* b1; int ld; };

template <int MODE>
struct Units;

template <int MODE>
DI void gemm_core(const u16* __restrict__ Ap, int lda, int u_begin, int u_end,
                  const u16* __restrict__ W0, const u16* __restrict__ W1, const u16* __restrict__ W2,
                  char* __restrict__ ws, const u16* __restrict__ xin, u16* __restrict__ out,
                  int row0, int kslice, char* lds) {
  const int tid = threadIdx.x, lane = tid & 63, r32 = lane & 31, hi = lane >> 5;
  const int wid = __builtin_amdgcn_readfirstlane(tid >> 6);
  const unsigned lds0 = (unsigned)(uintptr_t)lds;
  const int nchunk = (u_end - u_begin) * 3;

  const int st_tile = wid >> 2, st_rb = 3 * (wid & 3);
  auto issue = [&](int c) {
    const int u = u_begin + c / 3, kc = c % 3;
    const u16* base; int ld;
    if (MODE == 0) {
      if (u < 144) { base = (st_tile ? W2 : W1) + 32 * u; ld = F_; }
      else { base = W0 + 64 * (u - 144) + 32 * st_tile; ld = 3 * D_; }
    } else if (MODE == 1) {
      base = W0 + (size_t)kslice * 576 * D_ + 64 * u + 32 * st_tile; ld = D_;
    } else {
      base = W0 + 64 * u + 32 * st_tile; ld = D_;
    }
    const u16* src = base + (size_t)(kc * 192 + st_rb * 16 + (lane >> 2)) * ld + (lane & 3) * 8;
    const unsigned dst = lds0 + (c % NSLOT) * SLOT_B + st_tile * 12288 + st_rb * 1024;
#pragma unroll
    for (int i = 0; i < 3; ++i) glds16(src + (size_t)i * 16 * ld, (unsigned)__builtin_amdgcn_readfirstlane(dst + i * 1024));
  };
#pragma unroll
  for (int c = 0; c < NSLOT - 1; ++c) if (c < nchunk) issue(c);

  bf16x8 A[36];
  {
    const u16* ar = Ap + (size_t)(row0 + wid * 32 + r32) * lda + 8 * hi;
#pragma unroll
    for (int s = 0; s < 36; ++s) A[s] = *(const bf16x8*)(ar + 16 * s);
  }
  asm volatile("s_waitcnt vmcnt(0)" ::: "memory");

  const lds_cptr rd0 = (lds_cptr)lds + (hi * 8 + ((lane & 15) >> 2)) * 64 + ((lane >> 4) & 1) * 32 + (lane & 3) * 8;
  const int m = row0 + wid * 32 + r32;

  int c = 0;
  for (int u = u_begin; u < u_end; ++u) {
    f32x16 acc0 = {}, acc1 = {};
#pragma unroll
    for (int kc = 0; kc < 3; ++kc) {
      const int rem = nchunk - 1 - c;
      if (rem >= 2) { WAIT_BAR(6); } else if (rem == 1) { WAIT_BAR(3); } else { WAIT_BAR(0); }
      if (c + NSLOT - 1 < nchunk) issue(c + NSLOT - 1);
      const lds_cptr p = rd0 + (c % NSLOT) * SLOT_B;
#pragma unroll
      for (int ks = 0; ks < 12; ++ks) {
        bf16x8 b0 = wfrag(p + ks * 1024);
        bf16x8 b1 = wfrag(p + 12288 + ks * 1024);
        acc0 = MFMA(b0, A[kc * 12 + ks], acc0);
        acc1 = MFMA(b1, A[kc * 12 + ks], acc1);
      }
      ++c;
    }
    if (MODE == 0) {
      if (u < 144) {
        u16* hp = (u16*)(ws + WS_H) + (size_t)m * F_ + 32 * u + 4 * hi;
#pragma unroll
        for (int g = 0; g < 4; ++g) {
          float hv[4];
#pragma unroll
          for (int j = 0; j < 4; ++j) {
            const float gt = acc0[4 * g + j], up = acc1[4 * g + j];
            hv[j] = gt * up * __builtin_amdgcn_rcpf(1.0f + __builtin_amdgcn_exp2f(-1.4426950408889634f * gt));
          }
          *(u32x2*)(hp + 8 * g) = (u32x2){pack2(hv[0], hv[1]), pack2(hv[2], hv[3])};
        }
      } else {
        const int nb = 64 * (u - 144);
        const int voff = (((m >> 5) * 4 + ((m >> 4) & 1) * 2 + ((m >> 2) & 1)) * 20) * 8 + ((m >> 3) & 1) * 4 + (m & 3);
#pragma unroll
        for (int t = 0; t < 2; ++t) {
#pragma unroll
          for (int r = 0; r < 16; ++r) {
            const int n = nb + 32 * t + crow(r, hi);
            const float val = t ? acc1[r] : acc0[r];
            const int which = n / D_, f = n - which * D_, head = f / DH_, d = f - head * DH_;
            if (which == 0) {
              const u16 bv = f2bf(val * (0.23570226039551584f * 1.4426950408889634f));
              if (d < 16) ((u16*)(ws + WS_Q0))[(size_t)head * 32768 + (d >> 3) * 16384 + m * 8 + (d & 7)] = bv;
              else ((u16*)(ws + WS_Q1))[(size_t)head * 4096 + m * 2 + (d - 16)] = bv;
            } else if (which == 1) {
              u16* kv = (u16*)(ws + WS_KV + (size_t)head * KV_HEAD);
              const u16 bv = f2bf(val);
              if (d < 16) kv[(d >> 3) * 16384 + m * 8 + (d & 7)] = bv;
              else kv[KV_K1 / 2 + m * 2 + (d - 16)] = bv;
            } else {
              u16* kv = (u16*)(ws + WS_KV + (size_t)head * KV_HEAD + KV_V);
              kv[voff + d * 8] = f2bf(val);
            }
          }
        }
      }
    } else if (MODE == 1) {
      float* pp = (float*)(ws + WS_P) + ((size_t)kslice * S_ + m) * D_ + 64 * u + 4 * hi;
#pragma unroll
      for (int g = 0; g < 4; ++g) {
        *(f32x4*)(pp + 8 * g) = (f32x4){acc0[4 * g], acc0[4 * g + 1], acc0[4 * g + 2], acc0[4 * g + 3]};
        *(f32x4*)(pp + 32 + 8 * g) = (f32x4){acc1[4 * g], acc1[4 * g + 1], acc1[4 * g + 2], acc1[4 * g + 3]};
      }
    } else {
      const float* pp = (const float*)(ws + WS_P) + (size_t)m * D_ + 64 * u + 4 * hi;
#pragma unroll
      for (int t = 0; t < 2; ++t) {
#pragma unroll
        for (int g = 0; g < 4; ++g) {
          const int col = 64 * u + 32 * t + 8 * g + 4 * hi;
          f32x4 sum;
#pragma unroll
          for (int j = 0; j < 4; ++j) sum[j] = t ? acc1[4 * g + j] : acc0[4 * g + j];
#pragma unroll
          for (int s = 0; s < 8; ++s) sum += *(const f32x4*)(pp + (size_t)s * S_ * D_ + 32 * t + 8 * g);
          const u32x2 xv = *(const u32x2*)(xin + (size_t)m * D_ + col);
          sum[0] += bflo(xv[0]); sum[1] += bfhi(xv[0]); sum[2] += bflo(xv[1]); sum[3] += bfhi(xv[1]);
          *(u32x2*)(out + (size_t)m * D_ + col) = (u32x2){pack2(sum[0], sum[1]), pack2(sum[2], sum[3])};
        }
      }
    }
  }
}

__global__ __launch_bounds__(512, 2) void k_gemm1(const u16* __restrict__ wqkv, const u16* __restrict__ w1,
                                                  const u16* __restrict__ w3, char* __restrict__ ws) {
  extern __shared__ __attribute__((aligned(16))) char lds[];
  const int b = blockIdx.x, xcd = b & 7, idx = b >> 3;
  const int panel = idx & 7, cg = xcd * 4 + (idx >> 3);
  const int u0 = cg * 171 / 32, u1 = (cg + 1) * 171 / 32;
  gemm_core<0>((const u16*)(ws + WS_XN), D_, u0, u1, wqkv, w1, w3, ws, nullptr, nullptr, panel * 256, 0, lds);
}

__global__ __launch_bounds__(512, 2) void k_down(const u16* __restrict__ w2, char* __restrict__ ws) {
  extern __shared__ __attribute__((aligned(16))) char lds[];
  const int b = blockIdx.x, ks = b & 7, idx = b >> 3;
  const int panel = idx & 7, nt = idx >> 3;
  gemm_core<1>((const u16*)(ws + WS_H) + ks * 576, F_, nt * 3, nt * 3 + 3, w2, nullptr, nullptr, ws, nullptr, nullptr,
               panel * 256, ks, lds);
}

__global__ __launch_bounds__(512, 2) void k_final(const u16* __restrict__ wo, char* __restrict__ ws,
                                                  const u16* __restrict__ x, u16* __restrict__ out) {
  extern __shared__ __attribute__((aligned(16))) char lds[];
  const int b = blockIdx.x, panel = b & 7, u = b >> 3;
  gemm_core<2>((const u16*)(ws + WS_O), D_, u, u + 1, wo, nullptr, nullptr, ws, x, out, panel * 256, 0, lds);
}

constexpr int ATTN_LDS = (int)KV_HEAD;

__global__ __launch_bounds__(512, 2) void k_attn(char* __restrict__ ws) {
  extern __shared__ __attribute__((aligned(16))) char lds[];
  const int tid = threadIdx.x, lane = tid & 63, r32 = lane & 31, hi = lane >> 5;
  const int wid = __builtin_amdgcn_readfirstlane(tid >> 6);
  const int b = blockIdx.x, xcd = b & 7, idx = b >> 3;
  const int head = xcd * 4 + (idx >> 3), qb = idx & 7;
  const unsigned lds0 = (unsigned)(uintptr_t)lds;
  {
    const char* src = ws + WS_KV + (size_t)head * KV_HEAD + wid * 1024 + lane * 16;
#pragma unroll
    for (int i = 0; i < 19; ++i) glds16(src + i * 8192, (unsigned)__builtin_amdgcn_readfirstlane(lds0 + wid * 1024 + i * 8192));
  }
  const int q = qb * 256 + wid * 32 + r32;
  bf16x8 q0 = *(const bf16x8*)(ws + WS_Q0 + (size_t)head * 65536 + hi * 32768 + q * 16);
  unsigned q1w = *(const unsigned*)(ws + WS_Q1 + (size_t)head * 8192 + q * 4);
  if (hi) q1w = 0;
  const u32x4 q1u = {q1w, 0u, 0u, 0u};
  const bf16x8 q1 = __builtin_bit_cast(bf16x8, q1u);
  WAIT_BAR(0);

  const int dd = r32 < 20 ? r32 : r32 - 16;
  const lds_cptr kp0 = (lds_cptr)lds + hi * 32768 + r32 * 16;
  const lds_cptr kp1 = (lds_cptr)lds + KV_K1 + r32 * 4;
  const lds_cptr vp = (lds_cptr)lds + KV_V + (hi * 20 + dd) * 16;
  typedef __attribute__((address_space(3))) const bf16x8* lds_v8;
  typedef __attribute__((address_space(3))) const unsigned* lds_u;

  f32x16 negm;
  {
    bf16x8 kf0 = *(lds_v8)(kp0);
    const u32x4 k1u = {*(lds_u)(kp1), 0u, 0u, 0u};
    f32x16 s = {};
    s = MFMA(kf0, q0, s);
    s = MFMA(__builtin_bit_cast(bf16x8, k1u), q1, s);
    float mx = s[0];
#pragma unroll
    for (int r = 1; r < 16; ++r) mx = fmaxf(mx, s[r]);
    mx = fmaxf(mx, __shfl_xor(mx, 32));
#pragma unroll
    for (int r = 0; r < 16; ++r) negm[r] = -mx;
  }
  f32x16 o = {};
#pragma unroll 2
  for (int t = 0; t < 64; ++t) {
    bf16x8 kf0 = *(lds_v8)(kp0 + t * 512);
    const u32x4 k1u = {*(lds_u)(kp1 + t * 128), 0u, 0u, 0u};
    bf16x8 vf0 = *(lds_v8)(vp + t * 1280);
    bf16x8 vf1 = *(lds_v8)(vp + t * 1280 + 640);
    f32x16 s = MFMA(kf0, q0, negm);
    s = MFMA(__builtin_bit_cast(bf16x8, k1u), q1, s);
    u32x4 p0, p1;
#pragma unroll
    for (int j = 0; j < 4; ++j) {
      p0[j] = pack2(__builtin_amdgcn_exp2f(s[2 * j]), __builtin_amdgcn_exp2f(s[2 * j + 1]));
      p1[j] = pack2(__builtin_amdgcn_exp2f(s[8 + 2 * j]), __builtin_amdgcn_exp2f(s[9 + 2 * j]));
    }
    o = MFMA(vf0, __builtin_bit_cast(bf16x8, p0), o);
    o = MFMA(vf1, __builtin_bit_cast(bf16x8, p1), o);
  }
  float l = __shfl(o[10], r32);
  if (__any(!(l < 1.0e30f))) {
    float mrun = -INFINITY;
    o = (f32x16){};
    for (int t = 0; t < 64; ++t) {
      bf16x8 kf0 = *(lds_v8)(kp0 + t * 512);
      const u32x4 k1u = {*(lds_u)(kp1 + t * 128), 0u, 0u, 0u};
      bf16x8 vf0 = *(lds_v8)(vp + t * 1280);
      bf16x8 vf1 = *(lds_v8)(vp + t * 1280 + 640);
      f32x16 s = {};
      s = MFMA(kf0, q0, s);
      s = MFMA(__builtin_bit_cast(bf16x8, k1u), q1, s);
      float mx = s[0];
#pragma unroll
      for (int r = 1; r < 16; ++r) mx = fmaxf(mx, s[r]);
      mx = fmaxf(mx, __shfl_xor(mx, 32));
      const float mnew = fmaxf(mrun, mx);
      const float alpha = __builtin_amdgcn_exp2f(mrun - mnew);
      mrun = mnew;
#pragma unroll
      for (int r = 0; r < 16; ++r) o[r] *= alpha;
      u32x4 p0, p1;
#pragma unroll
      for (int j = 0; j < 4; ++j) {
        p0[j] = pack2(__builtin_amdgcn_exp2f(s[2 * j] - mnew), __builtin_amdgcn_exp2f(s[2 * j + 1] - mnew));
        p1[j] = pack2(__builtin_amdgcn_exp2f(s[8 + 2 * j] - mnew), __builtin_amdgcn_exp2f(s[9 + 2 * j] - mnew));
      }
      o = MFMA(vf0, __builtin_bit_cast(bf16x8, p0), o);
      o = MFMA(vf1, __builtin_bit_cast(bf16x8, p1), o);
    }
    l = __shfl(o[10], r32);
  }
  const float rl = 1.0f / l;
  u16* op = (u16*)(ws + WS_O) + (size_t)q * D_ + head * DH_ + 4 * hi;
  *(unsigned*)(op + 0) = pack2(o[0] * rl, o[1] * rl);
  *(unsigned*)(op + 2) = pack2(o[2] * rl, o[3] * rl);
  *(unsigned*)(op + 8) = pack2(o[4] * rl, o[5] * rl);
  *(unsigned*)(op + 10) = pack2(o[6] * rl, o[7] * rl);
  if (!hi) *(unsigned*)(op + 16) = pack2(o[8] * rl, o[9] * rl);
}

extern "C" void kernel_launch(void* const* d_in, const int* in_sizes, int n_in, void* d_out, int out_size,
                              void* d_ws, size_t ws_size, hipStream_t stream) {
  const u16* x = (const u16*)d_in[0];
  const u16* wqkv = (const u16*)d_in[1];
  const u16* wo = (const u16*)d_in[2];
  const u16* w1 = (const u16*)d_in[4];
  const u16* w3 = (const u16*)d_in[5];
  const u16* w2 = (const u16*)d_in[6];
  char* ws = (char*)d_ws;
  u16* out = (u16*)d_out;
  hipFuncSetAttribute((const void*)k_gemm1, hipFuncAttributeMaxDynamicSharedMemorySize, GEMM_LDS);
  hipFuncSetAttribute((const void*)k_down, hipFuncAttributeMaxDynamicSharedMemorySize, GEMM_LDS);
  hipFuncSetAttribute((const void*)k_final, hipFuncAttributeMaxDynamicSharedMemorySize, GEMM_LDS);
  hipFuncSetAttribute((const void*)k_attn, hipFuncAttributeMaxDynamicSharedMemorySize, ATTN_LDS);
  k_ln<<<512, 256, 0, stream>>>(x, ws);
  k_gemm1<<<256, 512, GEMM_LDS, stream>>>(wqkv, w1, w3, ws);
  k_attn<<<256, 512, ATTN_LDS, stream>>>(ws);
  k_down<<<192, 512, GEMM_LDS, stream>>>(w2, ws);
  k_final<<<72, 512, GEMM_LDS, stream>>>(wo, ws, x, out);
}
```

```cpp
#include <hip/hip_runtime.h>
#include <hip/hip_bf16.h>
#include <stdint.h>

typedef unsigned short u16;
typedef short bf16x8 __attribute__((ext_vector_type(8)));
typedef short s16x4 __attribute__((ext_vector_type(4)));
typedef float f32x16 __attribute__((ext_vector_type(16)));
typedef float f32x4 __attribute__((ext_vector_type(4)));
typedef float f32x2 __attribute__((ext_vector_type(2)));
typedef unsigned u32x4 __attribute__((ext_vector_type(4)));
typedef unsigned u32x2 __attribute__((ext_vector_type(2)));
typedef __bf16 bf16x2_t __attribute__((ext_vector_type(2)));
typedef short v4i16_t __attribute__((ext_vector_type(4)));
typedef __attribute__((address_space(3))) const char* lds_cptr;
typedef __attribute__((address_space(3))) char* lds_ptr;

#define DI __device__ __forceinline__
#define MFMA(a, b, c) __builtin_amdgcn_mfma_f32_32x32x16_bf16((a), (b), (c), 0, 0, 0)
#define SBAR() __builtin_amdgcn_sched_barrier(0)

constexpr int S_ = 2048, D_ = 576, H_ = 32, DH_ = 18, F_ = 4608;
constexpr size_t WS_XN = 0;
constexpr size_t WS_Q = WS_XN + (size_t)S_ * D_ * 2;
constexpr size_t QH = 81920;
constexpr size_t WS_KV = WS_Q + (size_t)H_ * QH;
constexpr size_t WS_H = WS_KV + (size_t)H_ * 2 * QH;
constexpr size_t WS_O = WS_H + (size_t)S_ * F_ * 2;
constexpr size_t WS_P = WS_O + (size_t)S_ * D_ * 2;
constexpr size_t WS_PS = WS_P + (size_t)8 * S_ * D_ * 4;
constexpr size_t WS_END = WS_PS + (size_t)S_ * D_ * 4;

#define STAMP(kid, i) do {} while (0)
DI unsigned pack2(float a, float b) {
  bf16x2_t r = __builtin_convertvector((f32x2){a, b}, bf16x2_t);
  return __builtin_bit_cast(unsigned, r);
}
DI void st16_wt(char* ws, size_t off, u32x4 v) {
  __builtin_amdgcn_raw_buffer_store_b128(v, __builtin_amdgcn_make_buffer_rsrc(ws, 0, 0x7fffffff, 0x00020000), (int)off, 0, 17);
}
DI float bflo(unsigned w) { return __uint_as_float(w << 16); }
DI float bfhi(unsigned w) { return __uint_as_float(w & 0xffff0000u); }

DI void glds16(const void* g, unsigned lds_base) {
  unsigned sv;
  asm volatile("s_mov_b32 %0, m0\n\ts_mov_b32 m0, %2\n\ts_nop 0\n\tglobal_load_lds_dwordx4 %1, off\n\ts_mov_b32 m0, %0"
               : "=&s"(sv) : "v"(g), "s"(lds_base) : "memory");
}
DI s16x4 vtr(lds_cptr p) {
  return __builtin_bit_cast(s16x4, __builtin_amdgcn_ds_read_tr16_b64_v4i16((__attribute__((address_space(3))) v4i16_t*)p));
}
template <int OFF2>
DI bf16x8 wfrag(lds_cptr p) {
  s16x4 lo = vtr(p), hi = vtr(p + OFF2);
  return (bf16x8){lo[0], lo[1], lo[2], lo[3], hi[0], hi[1], hi[2], hi[3]};
}
#define WAITV_BAR(N) asm volatile("s_waitcnt vmcnt(" #N ")\n\ts_barrier" ::: "memory")
#define WAITV(N) asm volatile("s_waitcnt vmcnt(" #N ")" ::: "memory")
#define WAITVN(N) asm volatile("s_waitcnt vmcnt(%0)" :: "i"(N) : "memory")
#define WAIT_BAR(N) asm volatile("s_waitcnt vmcnt(" #N ") lgkmcnt(0)\n\ts_barrier" ::: "memory")

__global__ __launch_bounds__(256) void k_ln(const u16* __restrict__ x, char* __restrict__ ws) {
  const int lane = threadIdx.x & 63, wid = threadIdx.x >> 6;
  const int row = (blockIdx.x * 4 + wid) * 8 + (lane >> 3), c8 = lane & 7;
  const u16* xr = x + (size_t)row * D_ + c8 * 8;
  u32x4 a[9];
#pragma unroll
  for (int j = 0; j < 9; ++j) a[j] = *(const u32x4*)(xr + 64 * j);
  float s = 0.f;
#pragma unroll
  for (int j = 0; j < 9; ++j)
#pragma unroll
    for (int i = 0; i < 4; ++i) s += bflo(a[j][i]) + bfhi(a[j][i]);
  s += __shfl_xor(s, 1); s += __shfl_xor(s, 2); s += __shfl_xor(s, 4);
  const float mean = s * (1.0f / D_);
  float q = 0.f;
#pragma unroll
  for (int j = 0; j < 9; ++j)
#pragma unroll
    for (int i = 0; i < 4; ++i) { float d0 = bflo(a[j][i]) - mean, d1 = bfhi(a[j][i]) - mean; q += d0 * d0 + d1 * d1; }
  q += __shfl_xor(q, 1); q += __shfl_xor(q, 2); q += __shfl_xor(q, 4);
  const float rstd = 1.0f / sqrtf(q * (1.0f / D_) + 1e-5f);
  char* xn = ws + WS_XN + ((size_t)(row >> 5) * 36 * 64 + (row & 31)) * 16;
#pragma unroll
  for (int j = 0; j < 9; ++j) {
    u32x4 o;
#pragma unroll
    for (int i = 0; i < 4; ++i) o[i] = pack2((bflo(a[j][i]) - mean) * rstd, (bfhi(a[j][i]) - mean) * rstd);
    const int c = c8 + 8 * j;
    *(u32x4*)(xn + ((c >> 1) * 64 + (c & 1) * 32) * 16) = o;
  }
}

constexpr int NSLOT = 4, SLOT_B = 36864;
constexpr int GEMM_LDS = NSLOT * SLOT_B + 16;

template <int V> struct IntC { static constexpr int value = V; };
#define PIN(x) asm volatile("" : "+v"(x))

template <int MODE, int NW>
DI void gemm_core(const char* __restrict__ Af  ,
                  int ngu, int gu0, int ndt, int dt0,
                  const u16* __restrict__ W0, const u16* __restrict__ W1, const u16* __restrict__ W2,
                  char* __restrict__ ws, const u16* __restrict__ xin, u16* __restrict__ out,
                  int rowblk  , int kslice, char* lds, int cnt_off) {
  const int tid = threadIdx.x, lane = tid & 63, r32 = lane & 31, hi = lane >> 5;
  const int wid = __builtin_amdgcn_readfirstlane(tid >> 6);
  const unsigned lds0 = (unsigned)(uintptr_t)lds;
  const int nchunk = 4 * ngu + 2 * ndt;
  STAMP(MODE, 0);
  typedef __attribute__((address_space(3))) volatile unsigned* lds_vu;
  const lds_vu cnt = (lds_vu)((lds_ptr)lds + cnt_off);
  if (tid == 0) *cnt = 0;

  auto chunk_src = [&](int c, int& ld) -> const u16* {
    const u16* base;
    if (MODE == 0) {
      if (c < 4 * ngu) { base = ((c & 2) ? W2 : W1) + 64 * (gu0 + (c >> 2)); ld = F_; }
      else { base = W0 + 64 * (dt0 + ((c - 4 * ngu) >> 1)); ld = 3 * D_; }
    } else if (MODE == 1) {
      base = W0 + (size_t)kslice * 576 * D_ + 64 * (dt0 + (c >> 1)); ld = D_;
    } else {
      base = W0 + 64 * (dt0 + (c >> 1)); ld = D_;
    }
    const int row = wid * 8 + (lane >> 3);
    return base + (size_t)((c & 1) * 288 + row) * ld + ((lane & 7) ^ (((lane >> 4) & 1) << 2)) * 8;
  };
  constexpr int NPIECE = (36 + NW - 1) / NW;
  auto issue = [&](int c) {
    int ld; const u16* src = chunk_src(c, ld);
    const unsigned dst = lds0 + (c % NSLOT) * SLOT_B + wid * 1024;
#pragma unroll
    for (int i = 0; i < NPIECE; ++i)
      if (i < 36 / NW || wid < 36 % NW)
        glds16(src + (size_t)i * NW * 8 * ld, (unsigned)__builtin_amdgcn_readfirstlane(dst + i * NW * 1024));
  };
#pragma unroll
  for (int c = 0; c < NSLOT; ++c) if (c < nchunk) issue(c);

  bf16x8 A[36];
#pragma unroll
  for (int s = 0; s < 36; ++s) A[s] = *(const bf16x8*)(Af + s * 1024 + lane * 16);
#pragma unroll
  for (int s = 0; s < 36; ++s) PIN(A[s]);
  asm volatile("s_waitcnt vmcnt(0) lgkmcnt(0)\n\ts_barrier" ::: "memory");
  SBAR();
  STAMP(MODE, 1);

  constexpr int OFF2 = (MODE == 1) ? 1024 : 512;
  const int q4 = (lane & 15) >> 2;
  const int rdo = ((MODE == 1 ? hi * 4 : hi * 8) + q4) * 128 + ((q4 >> 1) << 6) + ((lane >> 4) & 1) * 32 + (lane & 3) * 8;
  const lds_cptr rdA = (lds_cptr)lds + rdo, rdB = (lds_cptr)lds + (rdo ^ 64);
  const int m = rowblk * 32 + r32;

  auto silu = [&](float g) -> float { return g * __builtin_amdgcn_rcpf(1.0f + __builtin_amdgcn_exp2f(-1.4426950408889634f * g)); };
  auto epi_h = [&](const u32x4& sa, const u32x4& sb, const f32x16& up, int t32) {
    const size_t off = WS_H + (((size_t)rowblk * 288 + 2 * t32) * 64 + lane) * 16;
    u32x4 w0, w1;
#pragma unroll
    for (int j = 0; j < 4; ++j) {
      w0[j] = pack2(bflo(sa[j]) * up[2 * j], bfhi(sa[j]) * up[2 * j + 1]);
      w1[j] = pack2(bflo(sb[j]) * up[8 + 2 * j], bfhi(sb[j]) * up[9 + 2 * j]);
    }
    st16_wt(ws, off, w0);
    st16_wt(ws, off + 1024, w1);
  };
  auto epi_single = [&](const f32x16& a, int t) {
    if (MODE == 0) {
#pragma unroll
      for (int g = 0; g < 4; ++g)
#pragma unroll
        for (int pp = 0; pp < 2; ++pp) {
          const int n = 32 * t + 8 * g + 4 * hi + 2 * pp;
          const int which = n / D_, f = n - which * D_, head = f / DH_, d = f - head * DH_;
          const float sc = which == 0 ? (0.23570226039551584f * 1.4426950408889634f) : 1.0f;
          const size_t off = which == 0 ? (WS_Q + (size_t)head * QH) : (WS_KV + (size_t)head * 2 * QH + (size_t)(which - 1) * QH);
          *(unsigned*)(ws + off + (size_t)m * 40 + d * 2) = pack2(a[4 * g + 2 * pp] * sc, a[4 * g + 2 * pp + 1] * sc);
        }
    } else if (MODE == 1) {
      const size_t off = WS_P + (((((size_t)kslice * 64 + rowblk) * 18 + t) * 4) * 64 + lane) * 16;
#pragma unroll
      for (int g = 0; g < 4; ++g)
        st16_wt(ws, off + g * 1024, __builtin_bit_cast(u32x4, (f32x4){a[4 * g], a[4 * g + 1], a[4 * g + 2], a[4 * g + 3]}));
    } else {
      const char* pp = ws + WS_PS + ((((size_t)rowblk * 18 + t) * 4) * 64 + lane) * 16;
#pragma unroll
      for (int g = 0; g < 4; ++g) {
        const int col = 32 * t + 8 * g + 4 * hi;
        f32x4 sum = {a[4 * g], a[4 * g + 1], a[4 * g + 2], a[4 * g + 3]};
        sum += *(const f32x4*)(pp + g * 1024);
        const u32x2 xv = *(const u32x2*)(xin + (size_t)m * D_ + col);
        sum[0] += bflo(xv[0]); sum[1] += bfhi(xv[0]); sum[2] += bflo(xv[1]); sum[3] += bfhi(xv[1]);
        *(u32x2*)(out + (size_t)m * D_ + col) = (u32x2){pack2(sum[0], sum[1]), pack2(sum[2], sum[3])};
      }
    }
  };

  bf16x8 bq[6];
#pragma unroll
  for (int s = 0; s < 5; ++s) bq[s] = wfrag<OFF2>(rdA + s * 2048);

  int c = 0;
  auto run_chunk = [&](f32x16& aX, f32x16& aY, auto half_, auto sil_, auto nst_, f32x16& sg, u32x4& s0, u32x4& s1, int nrt) {
    constexpr int HALF = decltype(half_)::value, SIL = decltype(sil_)::value, NST = decltype(nst_)::value;
    int so = (c % NSLOT) * SLOT_B;
    const int cn = c + 1 < nchunk ? c + 1 : c;
    int sn = (cn % NSLOT) * SLOT_B;
    asm volatile("" : "+s"(so), "+s"(sn));
    const lds_cptr pn = rdA + sn;
    const bool doiss = c + 3 < nchunk && c >= 1;
    int nld; const u16* nsrc = chunk_src(doiss ? c + 3 : c, nld);
    const size_t nstr = (size_t)NW * 8 * nld;
    const unsigned ndst = (unsigned)__builtin_amdgcn_readfirstlane(lds0 + ((c + 3) % NSLOT) * SLOT_B + wid * 1024);
    const unsigned target = (unsigned)(NW * (c + 1));
    unsigned fl = 0;
    float sv[2];
#pragma unroll
    for (int s = 0; s < 36; ++s) {
      if (NW == 8 && s == 0) __builtin_amdgcn_s_setprio(2);
      if (NW == 8 && s == 18) __builtin_amdgcn_s_setprio(0);
      if (NW == 8 && s == 1) {
        PIN(aX);
        if (c >= 1) {
          if (c + 2 < nchunk) {
            if (NST >= 0) { if (wid < 4) { WAITVN(5 + (NST >= 0 ? NST : 0)); } else { WAITVN(4 + (NST >= 0 ? NST : 0)); } }
            else if (nrt == 16) { if (wid < 4) { WAITVN(21); } else { WAITVN(20); } }
            else { if (wid < 4) { WAITVN(9); } else { WAITVN(8); } }
          } else { WAITV(0); }
        }
        if (lane == 0) __hip_atomic_fetch_add((__attribute__((address_space(3))) unsigned*)((lds_ptr)lds + cnt_off), 1u, __ATOMIC_RELAXED, __HIP_MEMORY_SCOPE_WORKGROUP);
      }
      if (NW == 8 && s == 23) fl = *cnt;
      if (NW == 8 && s == 26) {
        PIN(aX); PIN(aY);
        while ((unsigned)__builtin_amdgcn_readfirstlane(fl) < target) { __builtin_amdgcn_s_sleep(1); fl = *cnt; }
        asm volatile("" ::: "memory");
      }
      if (NW == 8 && s >= 27 && (s - 27) % 2 == 0) {
        const int i = (s - 27) / 2;
        PIN(aY);
        if (doiss && (i < 4 || wid < 4)) glds16(nsrc + i * nstr, ndst + i * NW * 1024);
      }
      {
        const int sp = s + 5;
        if (sp < 18) bq[sp % 6] = wfrag<OFF2>(rdA + so + sp * 2048);
        else if (sp < 36) bq[sp % 6] = wfrag<OFF2>(rdB + so + (sp - 18) * 2048);
        else bq[sp % 6] = wfrag<OFF2>(pn + (sp - 36) * 2048);
      }
      if (s < 18) {
        if (HALF == 0 && s == 0) aX = MFMA(bq[0], A[0], (f32x16){});
        else aX = MFMA(bq[s % 6], A[18 * HALF + s], aX);
      } else {
        if (HALF == 0 && s == 18) aY = MFMA(bq[s % 6], A[0], (f32x16){});
        else aY = MFMA(bq[s % 6], A[18 * HALF + s - 18], aY);
      }
      if (SIL == 1 && s >= 19 && s < 35) {
        const int r = s - 19;
        sv[r & 1] = silu(aX[r]);
        if (r & 1) { unsigned w = pack2(sv[0], sv[1]); PIN(w); if (r < 8) s0[r >> 1] = w; else s1[(r - 8) >> 1] = w; }
      }
      if (SIL == 2 && s >= 1 && s < 17) {
        const int r = s - 1;
        sv[r & 1] = silu(sg[r]);
        if (r & 1) { unsigned w = pack2(sv[0], sv[1]); PIN(w); if (r < 8) s0[r >> 1] = w; else s1[(r - 8) >> 1] = w; }
      }
      SBAR();
    }
    ++c;
    STAMP(MODE, 1 + c);
  };

  f32x16 a0, a1;
  u32x4 sa0, sa1, sb0, sb1;
  if (MODE == 0) {
    for (int gi = 0; gi < ngu; ++gi) {
      run_chunk(a0, a1, IntC<0>{}, IntC<0>{}, IntC<4>{}, a1, sa0, sa1, 0);
      run_chunk(a0, a1, IntC<1>{}, IntC<1>{}, IntC<0>{}, a1, sa0, sa1, 0);
      run_chunk(a0, a1, IntC<0>{}, IntC<2>{}, IntC<0>{}, a1, sb0, sb1, 0);
      run_chunk(a0, a1, IntC<1>{}, IntC<0>{}, IntC<0>{}, a1, sb0, sb1, 0);
      epi_h(sa0, sa1, a0, 2 * (gu0 + gi));
      epi_h(sb0, sb1, a1, 2 * (gu0 + gi) + 1);
    }
    for (int di = 0; di < ndt; ++di) {
      run_chunk(a0, a1, IntC<0>{}, IntC<0>{}, IntC<-1>{}, a1, sa0, sa1, di ? 16 : 4);
      run_chunk(a0, a1, IntC<1>{}, IntC<0>{}, IntC<0>{}, a1, sa0, sa1, 0);
      epi_single(a0, 2 * (dt0 + di));
      epi_single(a1, 2 * (dt0 + di) + 1);
    }
  } else {
    for (int di = 0; di < ndt; ++di) {
      run_chunk(a0, a1, IntC<0>{}, IntC<0>{}, IntC<8>{}, a1, sa0, sa1, 0);
      run_chunk(a0, a1, IntC<1>{}, IntC<0>{}, IntC<0>{}, a1, sa0, sa1, 0);
      epi_single(a0, 2 * (dt0 + di));
      epi_single(a1, 2 * (dt0 + di) + 1);
    }
  }
  STAMP(MODE, 20);
}

__global__ __launch_bounds__(512, 2) void k_gemm1(const u16* __restrict__ wqkv, const u16* __restrict__ w1,
                                                  const u16* __restrict__ w3, char* __restrict__ ws) {
  extern __shared__ __attribute__((aligned(16))) char lds[];
  const int b = blockIdx.x, xcd = b & 7, idx = b >> 3;
  const int panel = idx & 7, cg = xcd * 4 + (idx >> 3);
  int ngu, gu0, ndt, dt0;
  if (cg < 8) { ngu = 3; gu0 = 3 * cg; ndt = 0; dt0 = 0; }
  else { const int j = cg - 8; ngu = 2; gu0 = 24 + 2 * j; ndt = j < 3 ? 2 : 1; dt0 = j < 3 ? 2 * j : 3 + j; }
  const int rowblk = panel * 8 + (threadIdx.x >> 6);
  gemm_core<0, 8>(ws + WS_XN + (size_t)rowblk * 36 * 1024, ngu, gu0, ndt, dt0, wqkv, w1, w3, ws, nullptr, nullptr,
                  rowblk, 0, lds, NSLOT * SLOT_B);
}

__global__ __launch_bounds__(512, 2) void k_down(const u16* __restrict__ w2, char* __restrict__ ws) {
  extern __shared__ __attribute__((aligned(16))) char lds[];
  const int b = blockIdx.x, ks = b & 7, idx = b >> 3;
  const int panel = idx & 7, nq = idx >> 3;
  const int ndt = nq == 0 ? 3 : 2, dt0 = nq == 0 ? 0 : 1 + 2 * nq;
  const int rowblk = panel * 8 + (threadIdx.x >> 6);
  gemm_core<1, 8>(ws + WS_H + ((size_t)rowblk * 288 + ks * 36) * 1024, 0, 0, ndt, dt0, w2, nullptr, nullptr, ws, nullptr,
                  nullptr, rowblk, ks, lds, NSLOT * SLOT_B);
}

__global__ __launch_bounds__(128, 2) void k_final(const u16* __restrict__ wo, char* __restrict__ ws,
                                                  const u16* __restrict__ x, u16* __restrict__ out) {
  extern __shared__ __attribute__((aligned(16))) char lds[];
  const int b = blockIdx.x, rp = b & 31, u = b >> 5;
  const int rowblk = rp * 2 + (threadIdx.x >> 6);
  gemm_core<2, 2>(ws + WS_O + (size_t)rowblk * 36 * 1024, 0, 0, 1, u, wo, nullptr, nullptr, ws, x, out, rowblk, 0, lds, 2 * SLOT_B);
}

constexpr int ATTN_LDS = 163840;

__global__ __launch_bounds__(768, 3) void k_attn(char* __restrict__ ws) {
  extern __shared__ __attribute__((aligned(16))) char lds[];
  const int tid = threadIdx.x, lane = tid & 63, r32 = lane & 31, hi = lane >> 5;
  const int wid = __builtin_amdgcn_readfirstlane(tid >> 6);
  const int b = blockIdx.x, xcd = b & 7, idx = b >> 3;
  const int head = xcd * 4 + (idx >> 3), qb = idx & 7;
  const unsigned lds0 = (unsigned)(uintptr_t)lds;
  STAMP(3, 0);
  if (wid >= 8) {
    WAITV_BAR(0);
    WAIT_BAR(0);
    for (int pc = wid - 8; pc < 18; pc += 4) {
      const size_t off = ((size_t)b * 18 + pc) * 1024 + lane * 16;
      f32x4 v[8];
#pragma unroll
      for (int sl = 0; sl < 8; ++sl) v[sl] = __builtin_nontemporal_load((const f32x4*)(ws + WS_P + (size_t)sl * (64 * 18 * 4 * 1024) + off));
      f32x4 sum = ((v[0] + v[1]) + (v[2] + v[3])) + ((v[4] + v[5]) + (v[6] + v[7]));
      *(f32x4*)(ws + WS_PS + off) = sum;
    }
    return;
  }
  {
    const char* src = ws + WS_KV + (size_t)head * 2 * QH + wid * 1024 + lane * 16;
#pragma unroll
    for (int i = 0; i < 20; ++i) glds16(src + i * 8192, (unsigned)__builtin_amdgcn_readfirstlane(lds0 + wid * 1024 + i * 8192));
  }
  const int q = qb * 256 + wid * 32 + r32;
  const char* qp = ws + WS_Q + (size_t)head * QH + (size_t)q * 40;
  u32x2 qa = *(const u32x2*)(qp + 16 * hi), qb2 = *(const u32x2*)(qp + 16 * hi + 8);
  unsigned q1w = *(const unsigned*)(qp + 32);
  asm volatile("" : "+v"(qa), "+v"(qb2), "+v"(q1w));
  WAITV_BAR(0);
  if (hi) q1w = 0;
  const bf16x8 q0 = __builtin_bit_cast(bf16x8, (u32x4){qa[0], qa[1], qb2[0], qb2[1]});
  const bf16x8 q1 = __builtin_bit_cast(bf16x8, (u32x4){q1w, 0u, 0u, 0u});
#pragma unroll
  for (int i = 0; i < 4; ++i) *(__attribute__((address_space(3))) unsigned*)((lds_ptr)lds + QH + (size_t)(tid + 512 * i) * 40 + 36) = 0x00003f80u;
  WAIT_BAR(0);
  STAMP(3, 1);

  typedef __attribute__((address_space(3))) const u32x2* lds_u2;
  typedef __attribute__((address_space(3))) const unsigned* lds_u;
  const lds_cptr kp = (lds_cptr)lds + r32 * 40 + 16 * hi;
  const lds_cptr kp1 = (lds_cptr)lds + r32 * 40 + 32;
  const lds_cptr vp = (lds_cptr)lds + QH + (4 * hi + ((lane & 15) >> 2)) * 40 + (((lane >> 4) & 1) ? 32 : (lane & 3) * 8);

#define LOADK(t, kf0, kf1)                                                                   \
  {                                                                                          \
    u32x2 a_ = *(lds_u2)(kp + (t) * 1280), b_ = *(lds_u2)(kp + (t) * 1280 + 8);              \
    kf0 = __builtin_bit_cast(bf16x8, (u32x4){a_[0], a_[1], b_[0], b_[1]});                   \
    kf1 = __builtin_bit_cast(bf16x8, (u32x4){*(lds_u)(kp1 + (t) * 1280), 0u, 0u, 0u});       \
  }
#define LOADV(t, vf0, vf1)                                                                   \
  {                                                                                          \
    vf0 = wfrag<320>(vp + (t) * 1280);                                                       \
    vf1 = wfrag<320>(vp + (t) * 1280 + 640);                                                 \
  }
#define EX(x) __builtin_amdgcn_exp2f(x)

  f32x16 negm;
  bf16x8 kf0, kf1, vf0, vf1;
  {
    LOADK(0, kf0, kf1);
    f32x16 s = MFMA(kf0, q0, (f32x16){});
    s = MFMA(kf1, q1, s);
    float mx = s[0];
#pragma unroll
    for (int r = 1; r < 16; ++r) mx = fmaxf(mx, s[r]);
    mx = fmaxf(mx, __shfl_xor(mx, 32));
#pragma unroll
    for (int r = 0; r < 16; ++r) negm[r] = -mx;
  }
  f32x16 o = {};
  f32x16 sA, sB;
  sA = MFMA(kf0, q0, negm);
  sA = MFMA(kf1, q1, sA);
  LOADK(1, kf0, kf1);
  LOADV(0, vf0, vf1);
#define STEP(SC, SN, t)                                                                                          \
  {                                                                                                              \
    u32x4 p0, p1;                                                                                                \
    SN = MFMA(kf0, q0, negm);                                                                                    \
    SC[0] = EX(SC[0]); SC[1] = EX(SC[1]); SC[2] = EX(SC[2]); SC[3] = EX(SC[3]);                                  \
    p0[0] = pack2(SC[0], SC[1]); p0[1] = pack2(SC[2], SC[3]); PIN(p0); SBAR();                                   \
    SN = MFMA(kf1, q1, SN);                                                                                      \
    { const int tn_ = (t) + 2 < 64 ? (t) + 2 : 63; LOADK(tn_, kf0, kf1); }                                       \
    SC[4] = EX(SC[4]); SC[5] = EX(SC[5]); SC[6] = EX(SC[6]); SC[7] = EX(SC[7]);                                  \
    p0[2] = pack2(SC[4], SC[5]); p0[3] = pack2(SC[6], SC[7]); PIN(p0); SBAR();                                   \
    SC[8] = EX(SC[8]); SC[9] = EX(SC[9]);                                                                        \
    o = MFMA(vf0, __builtin_bit_cast(bf16x8, p0), o);                                                            \
    SC[10] = EX(SC[10]); SC[11] = EX(SC[11]);                                                                    \
    p1[0] = pack2(SC[8], SC[9]); p1[1] = pack2(SC[10], SC[11]); PIN(p1); SBAR();                                 \
    SC[12] = EX(SC[12]); SC[13] = EX(SC[13]); SC[14] = EX(SC[14]); SC[15] = EX(SC[15]);                          \
    p1[2] = pack2(SC[12], SC[13]); p1[3] = pack2(SC[14], SC[15]); PIN(p1); SBAR();                               \
    o = MFMA(vf1, __builtin_bit_cast(bf16x8, p1), o);                                                            \
    { const int tn_ = (t) + 1 < 64 ? (t) + 1 : 63; LOADV(tn_, vf0, vf1); }                                       \
    SBAR();                                                                                                      \
  }
  for (int t = 0; t < 64; t += 2) {
    STEP(sA, sB, t);
    STEP(sB, sA, t + 1);
  }
  STAMP(3, 2);
  float l = __shfl(o[10], r32);
  if (__any(!(l < 1.0e30f))) {
    float mrun = -INFINITY;
    o = (f32x16){};
    for (int t = 0; t < 64; ++t) {
      LOADK(t, kf0, kf1);
      LOADV(t, vf0, vf1);
      f32x16 s = MFMA(kf0, q0, (f32x16){});
      s = MFMA(kf1, q1, s);
      float mx = s[0];
#pragma unroll
      for (int r = 1; r < 16; ++r) mx = fmaxf(mx, s[r]);
      mx = fmaxf(mx, __shfl_xor(mx, 32));
      const float mnew = fmaxf(mrun, mx);
      const float alpha = __builtin_amdgcn_exp2f(mrun - mnew);
      mrun = mnew;
#pragma unroll
      for (int r = 0; r < 16; ++r) o[r] *= alpha;
      u32x4 p0, p1;
#pragma unroll
      for (int j = 0; j < 4; ++j) {
        p0[j] = pack2(__builtin_amdgcn_exp2f(s[2 * j] - mnew), __builtin_amdgcn_exp2f(s[2 * j + 1] - mnew));
        p1[j] = pack2(__builtin_amdgcn_exp2f(s[8 + 2 * j] - mnew), __builtin_amdgcn_exp2f(s[9 + 2 * j] - mnew));
      }
      o = MFMA(vf0, __builtin_bit_cast(bf16x8, p0), o);
      o = MFMA(vf1, __builtin_bit_cast(bf16x8, p1), o);
    }
    l = __shfl(o[10], r32);
  }
  const float rl = 1.0f / l;
  char* ob = ws + WS_O + ((size_t)(q >> 5) * 36 * 64 + (q & 31)) * 16;
#pragma unroll
  for (int i = 0; i < 5; ++i) {
    const int d = (i >> 1) * 8 + 4 * hi + (i & 1) * 2;
    const int r = (i >> 1) * 4 + (i & 1) * 2;
    const int f = head * DH_ + d;
    if (i < 4 || !hi) *(unsigned*)(ob + ((f >> 4) * 64 + ((f >> 3) & 1) * 32) * 16 + (f & 7) * 2) = pack2(o[r] * rl, o[r + 1] * rl);
  }
  STAMP(3, 20);
}

extern "C" void kernel_launch(void* const* d_in, const int* in_sizes, int n_in, void* d_out, int out_size,
                              void* d_ws, size_t ws_size, hipStream_t stream) {
  const u16* x = (const u16*)d_in[0];
  const u16* wqkv = (const u16*)d_in[1];
  const u16* wo = (const u16*)d_in[2];
  const u16* w1 = (const u16*)d_in[4];
  const u16* w3 = (const u16*)d_in[5];
  const u16* w2 = (const u16*)d_in[6];
  char* ws = (char*)d_ws;
  u16* out = (u16*)d_out;
  (void)hipFuncSetAttribute((const void*)k_gemm1, hipFuncAttributeMaxDynamicSharedMemorySize, GEMM_LDS);
  (void)hipFuncSetAttribute((const void*)k_down, hipFuncAttributeMaxDynamicSharedMemorySize, GEMM_LDS);
  (void)hipFuncSetAttribute((const void*)k_final, hipFuncAttributeMaxDynamicSharedMemorySize, GEMM_LDS);
  (void)hipFuncSetAttribute((const void*)k_attn, hipFuncAttributeMaxDynamicSharedMemorySize, ATTN_LDS);
  k_ln<<<64, 256, 0, stream>>>(x, ws);
  k_gemm1<<<256, 512, GEMM_LDS, stream>>>(wqkv, w1, w3, ws);
  k_down<<<256, 512, GEMM_LDS, stream>>>(w2, ws);
  k_attn<<<256, 768, ATTN_LDS, stream>>>(ws);
  k_final<<<288, 128, 2 * SLOT_B + 16, stream>>>(wo, ws, x, out);
}
```

```cpp
#include <hip/hip_runtime.h>
#include <hip/hip_bf16.h>
#include <stdint.h>

typedef unsigned short u16;
typedef short bf16x8 __attribute__((ext_vector_type(8)));
typedef short s16x4 __attribute__((ext_vector_type(4)));
typedef float f32x16 __attribute__((ext_vector_type(16)));
typedef float f32x4 __attribute__((ext_vector_type(4)));
typedef float f32x2 __attribute__((ext_vector_type(2)));
typedef unsigned u32x4 __attribute__((ext_vector_type(4)));
typedef unsigned u32x2 __attribute__((ext_vector_type(2)));
typedef __bf16 bf16x2_t __attribute__((ext_vector_type(2)));
typedef short v4i16_t __attribute__((ext_vector_type(4)));
typedef __attribute__((address_space(3))) const char* lds_cptr;
typedef __attribute__((address_space(3))) char* lds_ptr;

#define DI __device__ __forceinline__
#define MFMA(a, b, c) __builtin_amdgcn_mfma_f32_32x32x16_bf16((a), (b), (c), 0, 0, 0)
#define SBAR() __builtin_amdgcn_sched_barrier(0)

constexpr int S_ = 2048, D_ = 576, H_ = 32, DH_ = 18, F_ = 4608;
constexpr size_t WS_XN = 0;
constexpr size_t WS_Q = WS_XN + (size_t)S_ * D_ * 2;
constexpr size_t QH = 81920;
constexpr size_t WS_KV = WS_Q + (size_t)H_ * QH;
constexpr size_t WS_H = WS_KV + (size_t)H_ * 2 * QH;
constexpr size_t WS_O = WS_H + (size_t)S_ * F_ * 2;
constexpr size_t WS_P = WS_O + (size_t)S_ * D_ * 2;
constexpr size_t WS_PS = WS_P + (size_t)8 * S_ * D_ * 4;
constexpr size_t WS_END = WS_PS + (size_t)S_ * D_ * 4;

#define STAMP(kid, i) do {} while (0)
DI unsigned pack2(float a, float b) {
  bf16x2_t r = __builtin_convertvector((f32x2){a, b}, bf16x2_t);
  return __builtin_bit_cast(unsigned, r);
}
DI void st16_wt(char* ws, size_t off, u32x4 v) {
  __builtin_amdgcn_raw_buffer_store_b128(v, __builtin_amdgcn_make_buffer_rsrc(ws, 0, 0x7fffffff, 0x00020000), (int)off, 0, 17);
}
DI float bflo(unsigned w) { return __uint_as_float(w << 16); }
DI float bfhi(unsigned w) { return __uint_as_float(w & 0xffff0000u); }

DI void glds16(const void* g, unsigned lds_base) {
  unsigned sv;
  asm volatile("s_mov_b32 %0, m0\n\ts_mov_b32 m0, %2\n\ts_nop 0\n\tglobal_load_lds_dwordx4 %1, off\n\ts_mov_b32 m0, %0"
               : "=&s"(sv) : "v"(g), "s"(lds_base) : "memory");
}
DI s16x4 vtr(lds_cptr p) {
  return __builtin_bit_cast(s16x4, __builtin_amdgcn_ds_read_tr16_b64_v4i16((__attribute__((address_space(3))) v4i16_t*)p));
}
template <int OFF2>
DI bf16x8 wfrag(lds_cptr p) {
  s16x4 lo = vtr(p), hi = vtr(p + OFF2);
  return (bf16x8){lo[0], lo[1], lo[2], lo[3], hi[0], hi[1], hi[2], hi[3]};
}
#define WAITV_BAR(N) asm volatile("s_waitcnt vmcnt(" #N ")\n\ts_barrier" ::: "memory")
#define WAITV(N) asm volatile("s_waitcnt vmcnt(" #N ")" ::: "memory")
#define WAITVN(N) asm volatile("s_waitcnt vmcnt(%0)" :: "i"(N) : "memory")
#define WAIT_BAR(N) asm volatile("s_waitcnt vmcnt(" #N ") lgkmcnt(0)\n\ts_barrier" ::: "memory")

__global__ __launch_bounds__(64) void k_ln(const u16* __restrict__ x, char* __restrict__ ws) {
  const int lane = threadIdx.x & 63, wid = threadIdx.x >> 6;
  const int row = (blockIdx.x + wid) * 8 + (lane >> 3), c8 = lane & 7;
  const u16* xr = x + (size_t)row * D_ + c8 * 8;
  u32x4 a[9];
#pragma unroll
  for (int j = 0; j < 9; ++j) a[j] = *(const u32x4*)(xr + 64 * j);
  float s = 0.f;
#pragma unroll
  for (int j = 0; j < 9; ++j)
#pragma unroll
    for (int i = 0; i < 4; ++i) s += bflo(a[j][i]) + bfhi(a[j][i]);
  s += __shfl_xor(s, 1); s += __shfl_xor(s, 2); s += __shfl_xor(s, 4);
  const float mean = s * (1.0f / D_);
  float q = 0.f;
#pragma unroll
  for (int j = 0; j < 9; ++j)
#pragma unroll
    for (int i = 0; i < 4; ++i) { float d0 = bflo(a[j][i]) - mean, d1 = bfhi(a[j][i]) - mean; q += d0 * d0 + d1 * d1; }
  q += __shfl_xor(q, 1); q += __shfl_xor(q, 2); q += __shfl_xor(q, 4);
  const float rstd = 1.0f / sqrtf(q * (1.0f / D_) + 1e-5f);
  char* xn = ws + WS_XN + ((size_t)(row >> 5) * 36 * 64 + (row & 31)) * 16;
#pragma unroll
  for (int j = 0; j < 9; ++j) {
    u32x4 o;
#pragma unroll
    for (int i = 0; i < 4; ++i) o[i] = pack2((bflo(a[j][i]) - mean) * rstd, (bfhi(a[j][i]) - mean) * rstd);
    const int c = c8 + 8 * j;
    *(u32x4*)(xn + ((c >> 1) * 64 + (c & 1) * 32) * 16) = o;
  }
}

constexpr int NSLOT = 4, SLOT_B = 36864;
constexpr int GEMM_LDS = NSLOT * SLOT_B + 16;

template <int V> struct IntC { static constexpr int value = V; };
#define PIN(x) asm volatile("" : "+v"(x))

template <int MODE, int NW>
DI void gemm_core(const char* __restrict__ Af  ,
                  int ngu, int gu0, int ndt, int dt0,
                  const u16* __restrict__ W0, const u16* __restrict__ W1, const u16* __restrict__ W2,
                  char* __restrict__ ws, const u16* __restrict__ xin, u16* __restrict__ out,
                  int rowblk  , int kslice, char* lds, int cnt_off, int tail) {
  const int tid = threadIdx.x, lane = tid & 63, r32 = lane & 31, hi = lane >> 5;
  const int wid = __builtin_amdgcn_readfirstlane(tid >> 6);
  const unsigned lds0 = (unsigned)(uintptr_t)lds;
  const int nchunk = 4 * ngu + 2 * ndt + (tail >= 0 ? 2 : 0);
  STAMP(MODE, 0);
  typedef __attribute__((address_space(3))) volatile unsigned* lds_vu;
  const lds_vu cnt = (lds_vu)((lds_ptr)lds + cnt_off);
  if (tid == 0) *cnt = 0;

  auto chunk_src = [&](int c, int& ld) __attribute__((always_inline)) -> const u16* {
    const u16* base;
    if (MODE == 0) {
      if (c < 4 * ngu) { base = ((c & 2) ? W2 : W1) + 64 * (gu0 + (c >> 2)); ld = F_; }
      else { base = W0 + 64 * (dt0 + ((c - 4 * ngu) >> 1)); ld = 3 * D_; }
    } else if (MODE == 1) {
      base = W0 + (size_t)kslice * 576 * D_ + 64 * ((c >> 1) < ndt ? dt0 + (c >> 1) : (tail >> 1)); ld = D_;
    } else {
      base = W0 + 64 * (dt0 + (c >> 1)); ld = D_;
    }
    const int row = wid * 8 + (lane >> 3);
    return base + (size_t)((c & 1) * 288 + row) * ld + ((lane & 7) ^ (((lane >> 4) & 1) << 2)) * 8;
  };
  constexpr int NPIECE = (36 + NW - 1) / NW;
  auto issue = [&](int c) __attribute__((always_inline)) {
    int ld; const u16* src = chunk_src(c, ld);
    const unsigned dst = lds0 + (c % NSLOT) * SLOT_B + wid * 1024;
#pragma unroll
    for (int i = 0; i < NPIECE; ++i)
      if (i < 36 / NW || wid < 36 % NW)
        glds16(src + (size_t)i * NW * 8 * ld, (unsigned)__builtin_amdgcn_readfirstlane(dst + i * NW * 1024));
  };
  u32x2 pfx[8], pfm[8];
  if (MODE == 2) {
#pragma unroll
    for (int i = 0; i < 8; ++i) {
      const int t = 2 * dt0 + (i >> 2), g = i & 3;
      pfx[i] = *(const u32x2*)(xin + (size_t)(rowblk * 32 + r32) * D_ + 32 * t + 8 * g + 4 * hi);
      pfm[i] = *(const u32x2*)(ws + WS_PS + ((((size_t)rowblk * 18 + t) * 4 + g) * 64 + lane) * 8);
    }
  }
  issue(0);
  issue(1);
  bf16x8 A[36];
  const char* ap = Af + lane * 16;
#define ALOAD(k) asm volatile("global_load_dwordx4 %0, %1, off offset:%2" : "=v"(A[k]) : "v"(ap + ((k) >> 2) * 4096), "i"(((k) & 3) * 1024) : "memory")
#pragma unroll
  for (int s = 0; s < 6; ++s) ALOAD(s);
  asm volatile("s_waitcnt vmcnt(%0) lgkmcnt(0)\n\ts_barrier" :: "i"(6 + (NW == 8 ? 4 : 18)) : "memory");
  SBAR();
  STAMP(MODE, 1);

  constexpr int OFF2 = (MODE == 1) ? 1024 : 512;
  const int q4 = (lane & 15) >> 2;
  const int rdo = ((MODE == 1 ? hi * 4 : hi * 8) + q4) * 128 + ((q4 >> 1) << 6) + ((lane >> 4) & 1) * 32 + (lane & 3) * 8;
  const lds_cptr rdA = (lds_cptr)lds + rdo, rdB = (lds_cptr)lds + (rdo ^ 64);
  const int dB = (rdo ^ 64) - rdo;
  const int m = rowblk * 32 + r32;

  auto silu = [&](float g) __attribute__((always_inline)) -> float { return g * __builtin_amdgcn_rcpf(1.0f + __builtin_amdgcn_exp2f(-1.4426950408889634f * g)); };
  auto epi_h = [&](const u32x4& sa, const u32x4& sb, const f32x16& up, int t32) __attribute__((always_inline)) {
    const size_t off = WS_H + (((size_t)rowblk * 288 + 2 * t32) * 64 + lane) * 16;
    u32x4 w0, w1;
#pragma unroll
    for (int j = 0; j < 4; ++j) {
      w0[j] = pack2(bflo(sa[j]) * up[2 * j], bfhi(sa[j]) * up[2 * j + 1]);
      w1[j] = pack2(bflo(sb[j]) * up[8 + 2 * j], bfhi(sb[j]) * up[9 + 2 * j]);
    }
    st16_wt(ws, off, w0);
    st16_wt(ws, off + 1024, w1);
  };
  auto epi_single = [&](const f32x16& a, int t) __attribute__((always_inline)) {
    if (MODE == 0) {
#pragma unroll
      for (int g = 0; g < 4; ++g)
#pragma unroll
        for (int pp = 0; pp < 2; ++pp) {
          const int n = 32 * t + 8 * g + 4 * hi + 2 * pp;
          const int which = n / D_, f = n - which * D_, head = f / DH_, d = f - head * DH_;
          const float sc = which == 0 ? (0.23570226039551584f * 1.4426950408889634f) : 1.0f;
          const size_t off = which == 0 ? (WS_Q + (size_t)head * QH) : (WS_KV + (size_t)head * 2 * QH + (size_t)(which - 1) * QH);
          *(unsigned*)(ws + off + (size_t)m * 40 + d * 2) = pack2(a[4 * g + 2 * pp] * sc, a[4 * g + 2 * pp + 1] * sc);
        }
    } else if (MODE == 1) {
      const size_t off = WS_P + (((((size_t)kslice * 64 + rowblk) * 18 + t) * 4) * 64 + lane) * 16;
#pragma unroll
      for (int g = 0; g < 4; ++g)
        st16_wt(ws, off + g * 1024, __builtin_bit_cast(u32x4, (f32x4){a[4 * g], a[4 * g + 1], a[4 * g + 2], a[4 * g + 3]}));
    } else {
#pragma unroll
      for (int g = 0; g < 4; ++g) {
        const int col = 32 * t + 8 * g + 4 * hi;
        const u32x2 xv = pfx[(t & 1) * 4 + g];
        const unsigned a01 = pack2(a[4 * g], a[4 * g + 1]), a23 = pack2(a[4 * g + 2], a[4 * g + 3]);
        const unsigned m01 = pfm[(t & 1) * 4 + g][0], m23 = pfm[(t & 1) * 4 + g][1];
        const unsigned t01 = pack2(bflo(xv[0]) + bflo(a01), bfhi(xv[0]) + bfhi(a01));
        const unsigned t23 = pack2(bflo(xv[1]) + bflo(a23), bfhi(xv[1]) + bfhi(a23));
        f32x4 sum = {bflo(t01) + bflo(m01), bfhi(t01) + bfhi(m01), bflo(t23) + bflo(m23), bfhi(t23) + bfhi(m23)};
        *(u32x2*)(out + (size_t)m * D_ + col) = (u32x2){pack2(sum[0], sum[1]), pack2(sum[2], sum[3])};
      }
    }
  };

  bf16x8 bq[6];
#pragma unroll
  for (int s = 0; s < 5; ++s) bq[s] = wfrag<OFF2>(rdA + s * 2048);

  int c = 0;
  auto run_chunk = [&](f32x16& aX, f32x16& aY, auto half_, auto sil_, auto nst_, auto start_, f32x16& sg, u32x4& s0, u32x4& s1, int nrt) __attribute__((always_inline)) {
    constexpr int HALF = decltype(half_)::value, SIL = decltype(sil_)::value, NST = decltype(nst_)::value, START = decltype(start_)::value;
    int so = __builtin_amdgcn_readfirstlane((c % NSLOT) * SLOT_B);
    const int cn = c + 1 < nchunk ? c + 1 : c;
    int sn = __builtin_amdgcn_readfirstlane((cn % NSLOT) * SLOT_B);
    asm volatile("" : "+s"(so), "+s"(sn));
    const lds_cptr pn = rdA + sn + ((MODE == 1 && tail >= 0 && c + 3 == nchunk && (tail & 1)) ? dB : 0);
    const bool doiss = START != 1 && c + 3 < nchunk;
    int nld; const u16* nsrc = chunk_src(doiss ? c + 3 : c, nld);
    const size_t nstr = (size_t)NW * 8 * nld;
    const unsigned ndst = (unsigned)__builtin_amdgcn_readfirstlane(lds0 + ((c + 3) % NSLOT) * SLOT_B + wid * 1024);
    const unsigned target = (unsigned)(NW * (c + 1));
    unsigned fl = 0;
    float sv[2], e1[2], e2[2];
#pragma unroll
    for (int s = 0; s < 36; ++s) {
      if (NW == 8 && s == 0) __builtin_amdgcn_s_setprio(2);
      if (NW == 8 && s == 18) __builtin_amdgcn_s_setprio(0);
      if (NW == 8 && s == (START == 2 ? 20 : 1)) {
        PIN(aX); PIN(aY);
        if (START == 2) {
          if (c + 2 < nchunk) { WAITV(4); } else { WAITV(0); }
        } else if (START == 0) {
          if (c + 2 < nchunk) {
            if (NST >= 0) { if (wid < 4) { WAITVN(5 + (NST >= 0 ? NST : 0)); } else { WAITVN(4 + (NST >= 0 ? NST : 0)); } }
            else if (nrt == 16) { if (wid < 4) { WAITVN(21); } else { WAITVN(20); } }
            else { if (wid < 4) { WAITVN(9); } else { WAITVN(8); } }
          } else { WAITV(0); }
        }
        if (lane == 0) __hip_atomic_fetch_add((__attribute__((address_space(3))) unsigned*)((lds_ptr)lds + cnt_off), 1u, __ATOMIC_RELAXED, __HIP_MEMORY_SCOPE_WORKGROUP);
      }
      if (NW != 8 && START == 1 && s == 26) { PIN(aX); PIN(aY); asm volatile("s_barrier" ::: "memory"); }
      if (NW == 8 && s == 23) fl = *cnt;
      if (NW == 8 && s == 26) {
        PIN(aX); PIN(aY);
        while ((unsigned)__builtin_amdgcn_readfirstlane(fl) < target) { __builtin_amdgcn_s_sleep(1); fl = *cnt; }
        asm volatile("" ::: "memory");
      }
      if (NW == 8 && s >= 27 && (s - 27) % 2 == 0) {
        const int i = (s - 27) / 2;
        PIN(aY);
        if (doiss && (i < 4 || wid < 4)) glds16(nsrc + i * nstr, ndst + i * NW * 1024);
      }
      {
        const int sp = s + 5;
        if (sp < 18) bq[sp % 6] = wfrag<OFF2>(rdA + so + sp * 2048);
        else if (sp < 36) bq[sp % 6] = wfrag<OFF2>(rdB + so + (sp - 18) * 2048);
        else bq[sp % 6] = wfrag<OFF2>(pn + (sp - 36) * 2048);
      }
      if (START == 1 && s < 18) asm volatile("s_waitcnt vmcnt(%1)" : "+v"(A[s]) : "i"(s <= 15 ? 5 + s : 35 - s));
      if (START == 2 && s < 18) asm volatile("s_waitcnt vmcnt(%1)" : "+v"(A[18 + s]) : "i"(17 - s + (NW == 8 ? 8 : 0)));
      if (s < 18) {
        if (HALF == 0 && s == 0) aX = MFMA(bq[0], A[0], (f32x16){});
        else aX = MFMA(bq[s % 6], A[18 * HALF + s], aX);
      } else {
        if (HALF == 0 && s == 18) aY = MFMA(bq[s % 6], A[0], (f32x16){});
        else aY = MFMA(bq[s % 6], A[18 * HALF + s - 18], aY);
      }
      if (START == 1) {
        if (s <= 14) { ALOAD(6 + 2 * s); ALOAD(7 + 2 * s); }
        if (NW == 8 && s >= 18 && s < 28) {
          const int cc = 2 + (s - 18) / 5, i = (s - 18) % 5;
          int ld2; const u16* src2 = chunk_src(cc, ld2);
          if (i < 4 || wid < 4) glds16(src2 + (size_t)i * NW * 8 * ld2, (unsigned)__builtin_amdgcn_readfirstlane(lds0 + cc * SLOT_B + wid * 1024 + i * NW * 1024));
        }
      }
      if (SIL != 0) {
        constexpr int B0 = (SIL == 1) ? 18 : 0;
        const f32x16& g = (SIL == 1) ? aX : sg;
        const int r1 = s - B0, r2 = s - B0 - 1, r3 = s - B0 - 2;
        if (r3 >= 0 && r3 < 16) {
          sv[r3 & 1] = g[r3] * e2[r3 & 1];
          if (r3 & 1) { unsigned w = pack2(sv[0], sv[1]); PIN(w); if (r3 < 8) s0[r3 >> 1] = w; else s1[(r3 - 8) >> 1] = w; }
        }
        if (r2 >= 0 && r2 < 16) { e2[r2 & 1] = __builtin_amdgcn_rcpf(1.0f + e1[r2 & 1]); PIN(e2[r2 & 1]); }
        if (r1 >= 0 && r1 < 16) { e1[r1 & 1] = __builtin_amdgcn_exp2f(-1.4426950408889634f * g[r1]); PIN(e1[r1 & 1]); }
      }
      SBAR();
    }
    ++c;
    STAMP(MODE, 1 + c);
  };

  auto run_tail = [&](f32x16& acc) __attribute__((always_inline)) {
    int so = __builtin_amdgcn_readfirstlane((c % NSLOT) * SLOT_B), sn = __builtin_amdgcn_readfirstlane(((c + 1) % NSLOT) * SLOT_B);
    asm volatile("" : "+s"(so), "+s"(sn));
    const lds_cptr rdO = rdA + ((tail & 1) ? dB : 0);
    const unsigned target = (unsigned)(NW * (c + 1));
    unsigned fl = 0;
#pragma unroll
    for (int s = 0; s < 36; ++s) {
      if (s == 1) {
        PIN(acc);
        WAITV(0);
        if (lane == 0) __hip_atomic_fetch_add((__attribute__((address_space(3))) unsigned*)((lds_ptr)lds + cnt_off), 1u, __ATOMIC_RELAXED, __HIP_MEMORY_SCOPE_WORKGROUP);
      }
      if (s == 9) fl = *cnt;
      if (s == 12) {
        PIN(acc);
        while ((unsigned)__builtin_amdgcn_readfirstlane(fl) < target) { __builtin_amdgcn_s_sleep(1); fl = *cnt; }
        asm volatile("" ::: "memory");
      }
      const int sp = s + 5;
      if (sp < 18) bq[sp % 6] = wfrag<OFF2>(rdO + so + sp * 2048);
      else if (sp < 36) bq[sp % 6] = wfrag<OFF2>(rdO + sn + (sp - 18) * 2048);
      if (s == 0) acc = MFMA(bq[0], A[0], (f32x16){});
      else acc = MFMA(bq[s % 6], A[s], acc);
      SBAR();
    }
  };

  f32x16 a0, a1;
  u32x4 sa0, sa1, sb0, sb1;
  typedef IntC<0> I0; typedef IntC<1> I1; typedef IntC<2> I2;
  if (MODE == 0) {
    run_chunk(a0, a1, I0{}, I0{}, I0{}, I1{}, a1, sa0, sa1, 0);
    run_chunk(a0, a1, I1{}, I1{}, I0{}, I2{}, a1, sa0, sa1, 0);
    for (int gi = 0; gi < ngu; ++gi) {
      run_chunk(a0, a1, I0{}, I2{}, I0{}, I0{}, a1, sb0, sb1, 0);
      run_chunk(a0, a1, I1{}, I0{}, I0{}, I0{}, a1, sb0, sb1, 0);
      epi_h(sa0, sa1, a0, 2 * (gu0 + gi));
      epi_h(sb0, sb1, a1, 2 * (gu0 + gi) + 1);
      if (gi + 1 < ngu) {
        run_chunk(a0, a1, I0{}, I0{}, IntC<4>{}, I0{}, a1, sa0, sa1, 0);
        run_chunk(a0, a1, I1{}, I1{}, I0{}, I0{}, a1, sa0, sa1, 0);
      }
    }
    for (int di = 0; di < ndt; ++di) {
      run_chunk(a0, a1, I0{}, I0{}, IntC<-1>{}, I0{}, a1, sa0, sa1, di ? 16 : 4);
      run_chunk(a0, a1, I1{}, I0{}, I0{}, I0{}, a1, sa0, sa1, 0);
      epi_single(a0, 2 * (dt0 + di));
      epi_single(a1, 2 * (dt0 + di) + 1);
    }
  } else {
    run_chunk(a0, a1, I0{}, I0{}, I0{}, I1{}, a1, sa0, sa1, 0);
    run_chunk(a0, a1, I1{}, I0{}, I0{}, I2{}, a1, sa0, sa1, 0);
    epi_single(a0, 2 * dt0);
    epi_single(a1, 2 * dt0 + 1);
    for (int di = 1; di < ndt; ++di) {
      run_chunk(a0, a1, I0{}, I0{}, IntC<8>{}, I0{}, a1, sa0, sa1, 0);
      run_chunk(a0, a1, I1{}, I0{}, I0{}, I0{}, a1, sa0, sa1, 0);
      epi_single(a0, 2 * (dt0 + di));
      epi_single(a1, 2 * (dt0 + di) + 1);
    }
    if (MODE == 1 && tail >= 0) {
      run_tail(a0);
      epi_single(a0, tail);
    }
  }
  STAMP(MODE, 20);
}

__global__ __launch_bounds__(512, 2) void k_gemm1(const u16* __restrict__ wqkv, const u16* __restrict__ w1,
                                                  const u16* __restrict__ w3, char* __restrict__ ws) {
  extern __shared__ __attribute__((aligned(16))) char lds[];
  const int b = blockIdx.x, xcd = b & 7, idx = b >> 3;
  const int panel = idx & 7, cg = xcd * 4 + (idx >> 3);
  int ngu, gu0, ndt, dt0;
  if (cg < 8) { ngu = 3; gu0 = 3 * cg; ndt = 0; dt0 = 0; }
  else { const int j = cg - 8; ngu = 2; gu0 = 24 + 2 * j; ndt = j < 3 ? 2 : 1; dt0 = j < 3 ? 2 * j : 3 + j; }
  const int rowblk = panel * 8 + (threadIdx.x >> 6);
  gemm_core<0, 8>(ws + WS_XN + (size_t)rowblk * 36 * 1024, ngu, gu0, ndt, dt0, wqkv, w1, w3, ws, nullptr, nullptr,
                  rowblk, 0, lds, NSLOT * SLOT_B, -1);
}

__global__ __launch_bounds__(512, 2) void k_down(const u16* __restrict__ w2, char* __restrict__ ws) {
  extern __shared__ __attribute__((aligned(16))) char lds[];
  const int b = blockIdx.x, ks = b & 7, idx = b >> 3;
  const int panel = idx & 7, nq = idx >> 3;
  const int ndt = 2, dt0 = 2 * nq, tail = nq < 2 ? 16 + nq : -1;
  const int rowblk = panel * 8 + (threadIdx.x >> 6);
  gemm_core<1, 8>(ws + WS_H + ((size_t)rowblk * 288 + ks * 36) * 1024, 0, 0, ndt, dt0, w2, nullptr, nullptr, ws, nullptr,
                  nullptr, rowblk, ks, lds, NSLOT * SLOT_B, tail);
}

__global__ __launch_bounds__(128, 2) void k_final(const u16* __restrict__ wo, char* __restrict__ ws,
                                                  const u16* __restrict__ x, u16* __restrict__ out) {
  extern __shared__ __attribute__((aligned(16))) char lds[];
  const int b = blockIdx.x, rp = b & 31, u = b >> 5;
  const int rowblk = rp * 2 + (threadIdx.x >> 6);
  gemm_core<2, 2>(ws + WS_O + (size_t)rowblk * 36 * 1024, 0, 0, 1, u, wo, nullptr, nullptr, ws, x, out, rowblk, 0, lds, 2 * SLOT_B, -1);
}

constexpr int ATTN_LDS = 163840;

__global__ __launch_bounds__(768, 3) void k_attn(char* __restrict__ ws) {
  extern __shared__ __attribute__((aligned(16))) char lds[];
  const int tid = threadIdx.x, lane = tid & 63, r32 = lane & 31, hi = lane >> 5;
  const int wid = __builtin_amdgcn_readfirstlane(tid >> 6);
  const int b = blockIdx.x, xcd = b & 7, idx = b >> 3;
  const int head = xcd * 4 + (idx >> 3), qb = idx & 7;
  const unsigned lds0 = (unsigned)(uintptr_t)lds;
  STAMP(3, 0);
  if (wid >= 8) {
    WAITV_BAR(0);
    WAIT_BAR(0);
    for (int pc = wid - 8; pc < 18; pc += 4) {
      const size_t off = ((size_t)b * 18 + pc) * 1024 + lane * 16;
      f32x4 v[8];
#pragma unroll
      for (int sl = 0; sl < 8; ++sl) v[sl] = __builtin_nontemporal_load((const f32x4*)(ws + WS_P + (size_t)sl * (64 * 18 * 4 * 1024) + off));
      f32x4 sum = ((v[0] + v[1]) + (v[2] + v[3])) + ((v[4] + v[5]) + (v[6] + v[7]));
      *(u32x2*)(ws + WS_PS + off / 2) = (u32x2){pack2(sum[0], sum[1]), pack2(sum[2], sum[3])};
    }
    return;
  }
  {
    const char* src = ws + WS_KV + (size_t)head * 2 * QH + wid * 1024 + lane * 16;
#pragma unroll
    for (int i = 0; i < 20; ++i) glds16(src + i * 8192, (unsigned)__builtin_amdgcn_readfirstlane(lds0 + wid * 1024 + i * 8192));
  }
  const int q = qb * 256 + wid * 32 + r32;
  const char* qp = ws + WS_Q + (size_t)head * QH + (size_t)q * 40;
  u32x2 qa = *(const u32x2*)(qp + 16 * hi), qb2 = *(const u32x2*)(qp + 16 * hi + 8);
  unsigned q1w = *(const unsigned*)(qp + 32);
  asm volatile("" : "+v"(qa), "+v"(qb2), "+v"(q1w));
  WAITV_BAR(0);
  if (hi) q1w = 0;
  const bf16x8 q0 = __builtin_bit_cast(bf16x8, (u32x4){qa[0], qa[1], qb2[0], qb2[1]});
  const bf16x8 q1 = __builtin_bit_cast(bf16x8, (u32x4){q1w, 0u, 0u, 0u});
#pragma unroll
  for (int i = 0; i < 4; ++i) *(__attribute__((address_space(3))) unsigned*)((lds_ptr)lds + QH + (size_t)(tid + 512 * i) * 40 + 36) = 0x00003f80u;
  WAIT_BAR(0);
  STAMP(3, 1);

  typedef __attribute__((address_space(3))) const u32x2* lds_u2;
  typedef __attribute__((address_space(3))) const unsigned* lds_u;
  const lds_cptr kp = (lds_cptr)lds + r32 * 40 + 16 * hi;
  const lds_cptr kp1 = (lds_cptr)lds + r32 * 40 + 32;
  const lds_cptr vp = (lds_cptr)lds + QH + (4 * hi + ((lane & 15) >> 2)) * 40 + (((lane >> 4) & 1) ? 32 : (lane & 3) * 8);

#define LOADK(t, kf0, kf1)                                                                   \
  {                                                                                          \
    u32x2 a_ = *(lds_u2)(kp + (t) * 1280), b_ = *(lds_u2)(kp + (t) * 1280 + 8);              \
    kf0 = __builtin_bit_cast(bf16x8, (u32x4){a_[0], a_[1], b_[0], b_[1]});                   \
    kf1 = __builtin_bit_cast(bf16x8, (u32x4){*(lds_u)(kp1 + (t) * 1280), 0u, 0u, 0u});       \
  }
#define LOADV(t, vf0, vf1)                                                                   \
  {                                                                                          \
    vf0 = wfrag<320>(vp + (t) * 1280);                                                       \
    vf1 = wfrag<320>(vp + (t) * 1280 + 640);                                                 \
  }
#define EX(x) __builtin_amdgcn_exp2f(x)

  f32x16 negm;
  bf16x8 kf0, kf1, vf0, vf1;
  {
    LOADK(0, kf0, kf1);
    f32x16 s = MFMA(kf0, q0, (f32x16){});
    s = MFMA(kf1, q1, s);
    float mx = s[0];
#pragma unroll
    for (int r = 1; r < 16; ++r) mx = fmaxf(mx, s[r]);
    mx = fmaxf(mx, __shfl_xor(mx, 32));
#pragma unroll
    for (int r = 0; r < 16; ++r) negm[r] = -mx;
  }
  f32x16 o = {};
  f32x16 sA, sB;
  sA = MFMA(kf0, q0, negm);
  sA = MFMA(kf1, q1, sA);
  LOADK(1, kf0, kf1);
  LOADV(0, vf0, vf1);
#define LOADK2(P0, P1, OFF)                                                                  \
  {                                                                                          \
    u32x2 a_ = *(lds_u2)((P0) + (OFF));                                                      \
    asm volatile("" ::: "memory");                                                           \
    u32x2 b_ = *(lds_u2)((P0) + (OFF) + 8);                                                  \
    kf0 = __builtin_bit_cast(bf16x8, (u32x4){a_[0], a_[1], b_[0], b_[1]});                   \
    kf1 = __builtin_bit_cast(bf16x8, (u32x4){*(lds_u)((P1) + (OFF)), 0u, 0u, 0u});           \
  }
#define STEP(SC, SN, KO, VO, NX, PK, PV)                                                                         \
  {                                                                                                              \
    u32x4 p0, p1;                                                                                                \
    if (NX) SN = MFMA(kf0, q0, negm);                                                                            \
    SC[0] = EX(SC[0]); SC[1] = EX(SC[1]); SC[2] = EX(SC[2]); SC[3] = EX(SC[3]);                                  \
    SC[4] = EX(SC[4]); SC[5] = EX(SC[5]);                                                                        \
    p0[0] = pack2(SC[0], SC[1]); p0[1] = pack2(SC[2], SC[3]); PIN(p0); SBAR();                                   \
    if (NX) SN = MFMA(kf1, q1, SN);                                                                              \
    if (PK) LOADK2(kq, kq1, KO);                                                                                 \
    SC[6] = EX(SC[6]); SC[7] = EX(SC[7]); SC[8] = EX(SC[8]); SC[9] = EX(SC[9]);                                  \
    p0[2] = pack2(SC[4], SC[5]); p0[3] = pack2(SC[6], SC[7]); PIN(p0); SBAR();                                   \
    SC[10] = EX(SC[10]); SC[11] = EX(SC[11]);                                                                    \
    o = MFMA(vf0, __builtin_bit_cast(bf16x8, p0), o);                                                            \
    SC[12] = EX(SC[12]); SC[13] = EX(SC[13]);                                                                    \
    p1[0] = pack2(SC[8], SC[9]); p1[1] = pack2(SC[10], SC[11]); PIN(p1); SBAR();                                 \
    SC[14] = EX(SC[14]); SC[15] = EX(SC[15]);                                                                    \
    p1[2] = pack2(SC[12], SC[13]); PIN(p1); SBAR();                                                              \
    p1[3] = pack2(SC[14], SC[15]); PIN(p1); SBAR();                                                              \
    o = MFMA(vf1, __builtin_bit_cast(bf16x8, p1), o);                                                            \
    if (PV) { vf0 = wfrag<320>(vq + (VO)); vf1 = wfrag<320>(vq + (VO) + 640); }                                  \
    SBAR();                                                                                                      \
  }
  {
    lds_cptr kq = kp + 2560, kq1 = kp1 + 2560, vq = vp + 1280;
    for (int t = 0; t < 62; t += 2) {
      PIN(vq); PIN(kq); PIN(kq1);
      STEP(sA, sB, 0, 0, true, true, true);
      STEP(sB, sA, 1280, 1280, true, true, true);
      kq += 2560; kq1 += 2560; vq += 2560;
    }
    STEP(sA, sB, 0, 0, true, false, true);
    STEP(sB, sA, 0, 0, false, false, false);
  }
  STAMP(3, 2);
  float l = __shfl(o[10], r32);
  if (__any(!(l < 1.0e30f))) {
    float mrun = -INFINITY;
    o = (f32x16){};
    for (int t = 0; t < 64; ++t) {
      LOADK(t, kf0, kf1);
      LOADV(t, vf0, vf1);
      f32x16 s = MFMA(kf0, q0, (f32x16){});
      s = MFMA(kf1, q1, s);
      float mx = s[0];
#pragma unroll
      for (int r = 1; r < 16; ++r) mx = fmaxf(mx, s[r]);
      mx = fmaxf(mx, __shfl_xor(mx, 32));
      const float mnew = fmaxf(mrun, mx);
      const float alpha = __builtin_amdgcn_exp2f(mrun - mnew);
      mrun = mnew;
#pragma unroll
      for (int r = 0; r < 16; ++r) o[r] *= alpha;
      u32x4 p0, p1;
#pragma unroll
      for (int j = 0; j < 4; ++j) {
        p0[j] = pack2(__builtin_amdgcn_exp2f(s[2 * j] - mnew), __builtin_amdgcn_exp2f(s[2 * j + 1] - mnew));
        p1[j] = pack2(__builtin_amdgcn_exp2f(s[8 + 2 * j] - mnew), __builtin_amdgcn_exp2f(s[9 + 2 * j] - mnew));
      }
      o = MFMA(vf0, __builtin_bit_cast(bf16x8, p0), o);
      o = MFMA(vf1, __builtin_bit_cast(bf16x8, p1), o);
    }
    l = __shfl(o[10], r32);
  }
  const float rl = 1.0f / l;
  char* ob = ws + WS_O + ((size_t)(q >> 5) * 36 * 64 + (q & 31)) * 16;
#pragma unroll
  for (int i = 0; i < 5; ++i) {
    const int d = (i >> 1) * 8 + 4 * hi + (i & 1) * 2;
    const int r = (i >> 1) * 4 + (i & 1) * 2;
    const int f = head * DH_ + d;
    if (i < 4 || !hi) *(unsigned*)(ob + ((f >> 4) * 64 + ((f >> 3) & 1) * 32) * 16 + (f & 7) * 2) = pack2(o[r] * rl, o[r + 1] * rl);
  }
  STAMP(3, 20);
}

extern "C" void kernel_launch(void* const* d_in, const int* in_sizes, int n_in, void* d_out, int out_size,
                              void* d_ws, size_t ws_size, hipStream_t stream) {
  const u16* x = (const u16*)d_in[0];
  const u16* wqkv = (const u16*)d_in[1];
  const u16* wo = (const u16*)d_in[2];
  const u16* w1 = (const u16*)d_in[4];
  const u16* w3 = (const u16*)d_in[5];
  const u16* w2 = (const u16*)d_in[6];
  char* ws = (char*)d_ws;
  u16* out = (u16*)d_out;
  (void)hipFuncSetAttribute((const void*)k_gemm1, hipFuncAttributeMaxDynamicSharedMemorySize, GEMM_LDS);
  (void)hipFuncSetAttribute((const void*)k_down, hipFuncAttributeMaxDynamicSharedMemorySize, GEMM_LDS);
  (void)hipFuncSetAttribute((const void*)k_final, hipFuncAttributeMaxDynamicSharedMemorySize, GEMM_LDS);
  (void)hipFuncSetAttribute((const void*)k_attn, hipFuncAttributeMaxDynamicSharedMemorySize, ATTN_LDS);
  k_ln<<<256, 64, 0, stream>>>(x, ws);
  k_gemm1<<<256, 512, GEMM_LDS, stream>>>(wqkv, w1, w3, ws);
  k_down<<<256, 512, GEMM_LDS, stream>>>(w2, ws);
  k_attn<<<256, 768, ATTN_LDS, stream>>>(ws);
  k_final<<<288, 128, 2 * SLOT_B + 16, stream>>>(wo, ws, x, out);
}
```

```cpp
#include <hip/hip_runtime.h>
#include <hip/hip_bf16.h>
#include <stdint.h>

typedef unsigned short u16;
typedef short bf16x8 __attribute__((ext_vector_type(8)));
typedef short s16x4 __attribute__((ext_vector_type(4)));
typedef float f32x16 __attribute__((ext_vector_type(16)));
typedef float f32x4 __attribute__((ext_vector_type(4)));
typedef float f32x2 __attribute__((ext_vector_type(2)));
typedef unsigned u32x4 __attribute__((ext_vector_type(4)));
typedef unsigned u32x2 __attribute__((ext_vector_type(2)));
typedef __bf16 bf16x2_t __attribute__((ext_vector_type(2)));
typedef short v4i16_t __attribute__((ext_vector_type(4)));
typedef __attribute__((address_space(3))) const char* lds_cptr;
typedef __attribute__((address_space(3))) char* lds_ptr;

#define DI __device__ __forceinline__
#define MFMA(a, b, c) __builtin_amdgcn_mfma_f32_32x32x16_bf16((a), (b), (c), 0, 0, 0)
#define SBAR() __builtin_amdgcn_sched_barrier(0)

constexpr int S_ = 2048, D_ = 576, H_ = 32, DH_ = 18, F_ = 4608;
constexpr size_t WS_XN = 0;
constexpr size_t WS_Q = WS_XN + (size_t)S_ * D_ * 2;
constexpr size_t QH = 81920;
constexpr size_t WS_KV = WS_Q + (size_t)H_ * QH;
constexpr size_t WS_H = WS_KV + (size_t)H_ * 2 * QH;
constexpr size_t WS_O = WS_H + (size_t)S_ * F_ * 2;
constexpr size_t WS_P = WS_O + (size_t)S_ * D_ * 2;
constexpr size_t WS_PS = WS_P + (size_t)8 * S_ * D_ * 4;
constexpr size_t WS_END = WS_PS + (size_t)S_ * D_ * 4;

#define STAMP(kid, i) do {} while (0)
DI unsigned pack2(float a, float b) {
  bf16x2_t r = __builtin_convertvector((f32x2){a, b}, bf16x2_t);
  return __builtin_bit_cast(unsigned, r);
}
DI void st16_wt(char* ws, size_t off, u32x4 v) {
  __builtin_amdgcn_raw_buffer_store_b128(v, __builtin_amdgcn_make_buffer_rsrc(ws, 0, 0x7fffffff, 0x00020000), (int)off, 0, 17);
}
DI float bflo(unsigned w) { return __uint_as_float(w << 16); }
DI float bfhi(unsigned w) { return __uint_as_float(w & 0xffff0000u); }

DI void glds16(const void* g, unsigned lds_base) {
  unsigned sv;
  asm volatile("s_mov_b32 %0, m0\n\ts_mov_b32 m0, %2\n\ts_nop 0\n\tglobal_load_lds_dwordx4 %1, off\n\ts_mov_b32 m0, %0"
               : "=&s"(sv) : "v"(g), "s"(lds_base) : "memory");
}
DI s16x4 vtr(lds_cptr p) {
  return __builtin_bit_cast(s16x4, __builtin_amdgcn_ds_read_tr16_b64_v4i16((__attribute__((address_space(3))) v4i16_t*)p));
}
template <int OFF2>
DI bf16x8 wfrag(lds_cptr p) {
  s16x4 lo = vtr(p), hi = vtr(p + OFF2);
  return (bf16x8){lo[0], lo[1], lo[2], lo[3], hi[0], hi[1], hi[2], hi[3]};
}
#define WAITV_BAR(N) asm volatile("s_waitcnt vmcnt(" #N ")\n\ts_barrier" ::: "memory")
#define WAITV(N) asm volatile("s_waitcnt vmcnt(" #N ")" ::: "memory")
#define WAITVN(N) asm volatile("s_waitcnt vmcnt(%0)" :: "i"(N) : "memory")
#define WAIT_BAR(N) asm volatile("s_waitcnt vmcnt(" #N ") lgkmcnt(0)\n\ts_barrier" ::: "memory")

__global__ __launch_bounds__(64) void k_ln(const u16* __restrict__ x, char* __restrict__ ws) {
  const int lane = threadIdx.x & 63, wid = threadIdx.x >> 6;
  const int row = (blockIdx.x + wid) * 8 + (lane >> 3), c8 = lane & 7;
  const u16* xr = x + (size_t)row * D_ + c8 * 8;
  u32x4 a[9];
#pragma unroll
  for (int j = 0; j < 9; ++j) a[j] = *(const u32x4*)(xr + 64 * j);
  float s = 0.f;
#pragma unroll
  for (int j = 0; j < 9; ++j)
#pragma unroll
    for (int i = 0; i < 4; ++i) s += bflo(a[j][i]) + bfhi(a[j][i]);
  s += __shfl_xor(s, 1); s += __shfl_xor(s, 2); s += __shfl_xor(s, 4);
  const float mean = s * (1.0f / D_);
  float q = 0.f;
#pragma unroll
  for (int j = 0; j < 9; ++j)
#pragma unroll
    for (int i = 0; i < 4; ++i) { float d0 = bflo(a[j][i]) - mean, d1 = bfhi(a[j][i]) - mean; q += d0 * d0 + d1 * d1; }
  q += __shfl_xor(q, 1); q += __shfl_xor(q, 2); q += __shfl_xor(q, 4);
  const float rstd = 1.0f / sqrtf(q * (1.0f / D_) + 1e-5f);
  char* xn = ws + WS_XN + ((size_t)(row >> 5) * 36 * 64 + (row & 31)) * 16;
#pragma unroll
  for (int j = 0; j < 9; ++j) {
    u32x4 o;
#pragma unroll
    for (int i = 0; i < 4; ++i) o[i] = pack2((bflo(a[j][i]) - mean) * rstd, (bfhi(a[j][i]) - mean) * rstd);
    const int c = c8 + 8 * j;
    *(u32x4*)(xn + ((c >> 1) * 64 + (c & 1) * 32) * 16) = o;
  }
}

constexpr int NSLOT = 4, SLOT_B = 36864;
constexpr int GEMM_LDS = NSLOT * SLOT_B + 16;

template <int V> struct IntC { static constexpr int value = V; };
#define PIN(x) asm volatile("" : "+v"(x))

template <int MODE, int NW>
DI void gemm_core(const char* __restrict__ Af  ,
                  int ngu, int gu0, int ndt, int dt0,
                  const u16* __restrict__ W0, const u16* __restrict__ W1, const u16* __restrict__ W2,
                  char* __restrict__ ws, const u16* __restrict__ xin, u16* __restrict__ out,
                  int rowblk  , int kslice, char* lds, int cnt_off, int tail) {
  const int tid = threadIdx.x, lane = tid & 63, r32 = lane & 31, hi = lane >> 5;
  const int wid = __builtin_amdgcn_readfirstlane(tid >> 6);
  const unsigned lds0 = (unsigned)(uintptr_t)lds;
  const int nchunk = 4 * ngu + 2 * ndt + (tail >= 0 ? 2 : 0);
  STAMP(MODE, 0);
  typedef __attribute__((address_space(3))) volatile unsigned* lds_vu;
  const lds_vu cnt = (lds_vu)((lds_ptr)lds + cnt_off);
  if (tid == 0) *cnt = 0;

  auto chunk_src = [&](int c, int& ld) __attribute__((always_inline)) -> const u16* {
    const u16* base;
    if (MODE == 0) {
      if (c < 4 * ngu) { base = ((c & 2) ? W2 : W1) + 64 * (gu0 + (c >> 2)); ld = F_; }
      else { base = W0 + 64 * (dt0 + ((c - 4 * ngu) >> 1)); ld = 3 * D_; }
    } else if (MODE == 1) {
      base = W0 + (size_t)kslice * 576 * D_ + 64 * ((c >> 1) < ndt ? dt0 + (c >> 1) : (tail >> 1)); ld = D_;
    } else {
      base = W0 + 64 * (dt0 + (c >> 1)); ld = D_;
    }
    const int row = wid * 8 + (lane >> 3);
    return base + (size_t)((c & 1) * 288 + row) * ld + ((lane & 7) ^ (((lane >> 4) & 1) << 2)) * 8;
  };
  constexpr int NPIECE = (36 + NW - 1) / NW;
  auto issue = [&](int c) __attribute__((always_inline)) {
    int ld; const u16* src = chunk_src(c, ld);
    const unsigned dst = lds0 + (c % NSLOT) * SLOT_B + wid * 1024;
#pragma unroll
    for (int i = 0; i < NPIECE; ++i)
      if (i < 36 / NW || wid < 36 % NW)
        glds16(src + (size_t)i * NW * 8 * ld, (unsigned)__builtin_amdgcn_readfirstlane(dst + i * NW * 1024));
  };
  u32x2 pfx[8]; f32x4 pfm[8];
  if (MODE == 2) {
#pragma unroll
    for (int i = 0; i < 8; ++i) {
      const int t = 2 * dt0 + (i >> 2), g = i & 3;
      pfx[i] = *(const u32x2*)(xin + (size_t)(rowblk * 32 + r32) * D_ + 32 * t + 8 * g + 4 * hi);
      pfm[i] = *(const f32x4*)(ws + WS_PS + ((((size_t)rowblk * 18 + t) * 4 + g) * 64 + lane) * 16);
    }
  }
  issue(0);
  issue(1);
  bf16x8 A[36];
  const char* ap = Af + lane * 16;
#define ALOAD(k) asm volatile("global_load_dwordx4 %0, %1, off offset:%2" : "=v"(A[k]) : "v"(ap + ((k) >> 2) * 4096), "i"(((k) & 3) * 1024) : "memory")
#pragma unroll
  for (int s = 0; s < 6; ++s) ALOAD(s);
  asm volatile("s_waitcnt vmcnt(%0) lgkmcnt(0)\n\ts_barrier" :: "i"(6 + (NW == 8 ? 4 : 18)) : "memory");
  SBAR();
  STAMP(MODE, 1);

  constexpr int OFF2 = (MODE == 1) ? 1024 : 512;
  const int q4 = (lane & 15) >> 2;
  const int rdo = ((MODE == 1 ? hi * 4 : hi * 8) + q4) * 128 + ((q4 >> 1) << 6) + ((lane >> 4) & 1) * 32 + (lane & 3) * 8;
  const lds_cptr rdA = (lds_cptr)lds + rdo, rdB = (lds_cptr)lds + (rdo ^ 64);
  const int dB = (rdo ^ 64) - rdo;
  const int m = rowblk * 32 + r32;

  auto silu = [&](float g) __attribute__((always_inline)) -> float { return g * __builtin_amdgcn_rcpf(1.0f + __builtin_amdgcn_exp2f(-1.4426950408889634f * g)); };
  auto epi_h = [&](const u32x4& sa, const u32x4& sb, const f32x16& up, int t32) __attribute__((always_inline)) {
    const size_t off = WS_H + (((size_t)rowblk * 288 + 2 * t32) * 64 + lane) * 16;
    u32x4 w0, w1;
#pragma unroll
    for (int j = 0; j < 4; ++j) {
      w0[j] = pack2(bflo(sa[j]) * up[2 * j], bfhi(sa[j]) * up[2 * j + 1]);
      w1[j] = pack2(bflo(sb[j]) * up[8 + 2 * j], bfhi(sb[j]) * up[9 + 2 * j]);
    }
    st16_wt(ws, off, w0);
    st16_wt(ws, off + 1024, w1);
  };
  auto epi_single = [&](const f32x16& a, int t) __attribute__((always_inline)) {
    if (MODE == 0) {
#pragma unroll
      for (int g = 0; g < 4; ++g)
#pragma unroll
        for (int pp = 0; pp < 2; ++pp) {
          const int n = 32 * t + 8 * g + 4 * hi + 2 * pp;
          const int which = n / D_, f = n - which * D_, head = f / DH_, d = f - head * DH_;
          const float sc = which == 0 ? (0.23570226039551584f * 1.4426950408889634f) : 1.0f;
          const size_t off = which == 0 ? (WS_Q + (size_t)head * QH) : (WS_KV + (size_t)head * 2 * QH + (size_t)(which - 1) * QH);
          *(unsigned*)(ws + off + (size_t)m * 40 + d * 2) = pack2(a[4 * g + 2 * pp] * sc, a[4 * g + 2 * pp + 1] * sc);
        }
    } else if (MODE == 1) {
      const size_t off = WS_P + (((((size_t)kslice * 64 + rowblk) * 18 + t) * 4) * 64 + lane) * 16;
#pragma unroll
      for (int g = 0; g < 4; ++g)
        st16_wt(ws, off + g * 1024, __builtin_bit_cast(u32x4, (f32x4){a[4 * g], a[4 * g + 1], a[4 * g + 2], a[4 * g + 3]}));
    } else {
#pragma unroll
      for (int g = 0; g < 4; ++g) {
        const int col = 32 * t + 8 * g + 4 * hi;
        const u32x2 xv = pfx[(t & 1) * 4 + g];
        const f32x4 pm = pfm[(t & 1) * 4 + g];
        f32x4 sum = {a[4 * g] + pm[0] + bflo(xv[0]), a[4 * g + 1] + pm[1] + bfhi(xv[0]),
                     a[4 * g + 2] + pm[2] + bflo(xv[1]), a[4 * g + 3] + pm[3] + bfhi(xv[1])};
        *(u32x2*)(out + (size_t)m * D_ + col) = (u32x2){pack2(sum[0], sum[1]), pack2(sum[2], sum[3])};
      }
    }
  };

  bf16x8 bq[6];
#pragma unroll
  for (int s = 0; s < 5; ++s) bq[s] = wfrag<OFF2>(rdA + s * 2048);

  int c = 0;
  auto run_chunk = [&](f32x16& aX, f32x16& aY, auto half_, auto sil_, auto nst_, auto start_, f32x16& sg, u32x4& s0, u32x4& s1, int nrt) __attribute__((always_inline)) {
    constexpr int HALF = decltype(half_)::value, SIL = decltype(sil_)::value, NST = decltype(nst_)::value, START = decltype(start_)::value;
    int so = __builtin_amdgcn_readfirstlane((c % NSLOT) * SLOT_B);
    const int cn = c + 1 < nchunk ? c + 1 : c;
    int sn = __builtin_amdgcn_readfirstlane((cn % NSLOT) * SLOT_B);
    asm volatile("" : "+s"(so), "+s"(sn));
    const lds_cptr pn = rdA + sn + ((MODE == 1 && tail >= 0 && c + 3 == nchunk && (tail & 1)) ? dB : 0);
    const bool doiss = START != 1 && c + 3 < nchunk;
    int nld; const u16* nsrc = chunk_src(doiss ? c + 3 : c, nld);
    const size_t nstr = (size_t)NW * 8 * nld;
    const unsigned ndst = (unsigned)__builtin_amdgcn_readfirstlane(lds0 + ((c + 3) % NSLOT) * SLOT_B + wid * 1024);
    const unsigned target = (unsigned)(NW * (c + 1));
    unsigned fl = 0;
    float sv[2], e1[2], e2[2];
#pragma unroll
    for (int s = 0; s < 36; ++s) {
      if (NW == 8 && s == 0) __builtin_amdgcn_s_setprio(2);
      if (NW == 8 && s == 18) __builtin_amdgcn_s_setprio(0);
      if (NW == 8 && s == (START == 2 ? 20 : 1)) {
        PIN(aX); PIN(aY);
        if (START == 2) {
          if (c + 2 < nchunk) { WAITV(4); } else { WAITV(0); }
        } else if (START == 0) {
          if (c + 2 < nchunk) {
            if (NST >= 0) { if (wid < 4) { WAITVN(5 + (NST >= 0 ? NST : 0)); } else { WAITVN(4 + (NST >= 0 ? NST : 0)); } }
            else if (nrt == 16) { if (wid < 4) { WAITVN(21); } else { WAITVN(20); } }
            else { if (wid < 4) { WAITVN(9); } else { WAITVN(8); } }
          } else { WAITV(0); }
        }
        if (lane == 0) __hip_atomic_fetch_add((__attribute__((address_space(3))) unsigned*)((lds_ptr)lds + cnt_off), 1u, __ATOMIC_RELAXED, __HIP_MEMORY_SCOPE_WORKGROUP);
      }
      if (NW != 8 && START == 1 && s == 26) { PIN(aX); PIN(aY); asm volatile("s_barrier" ::: "memory"); }
      if (NW == 8 && s == 23) fl = *cnt;
      if (NW == 8 && s == 26) {
        PIN(aX); PIN(aY);
        while ((unsigned)__builtin_amdgcn_readfirstlane(fl) < target) { __builtin_amdgcn_s_sleep(1); fl = *cnt; }
        asm volatile("" ::: "memory");
      }
      if (NW == 8 && s >= 27 && (s - 27) % 2 == 0) {
        const int i = (s - 27) / 2;
        PIN(aY);
        if (doiss && (i < 4 || wid < 4)) glds16(nsrc + i * nstr, ndst + i * NW * 1024);
      }
      {
        const int sp = s + 5;
        if (sp < 18) bq[sp % 6] = wfrag<OFF2>(rdA + so + sp * 2048);
        else if (sp < 36) bq[sp % 6] = wfrag<OFF2>(rdB + so + (sp - 18) * 2048);
        else bq[sp % 6] = wfrag<OFF2>(pn + (sp - 36) * 2048);
      }
      if (START == 1 && s < 18) asm volatile("s_waitcnt vmcnt(%1)" : "+v"(A[s]) : "i"(s <= 15 ? 5 + s : 35 - s));
      if (START == 2 && s < 18) asm volatile("s_waitcnt vmcnt(%1)" : "+v"(A[18 + s]) : "i"(17 - s + (NW == 8 ? 8 : 0)));
      if (s < 18) {
        if (HALF == 0 && s == 0) aX = MFMA(bq[0], A[0], (f32x16){});
        else aX = MFMA(bq[s % 6], A[18 * HALF + s], aX);
      } else {
        if (HALF == 0 && s == 18) aY = MFMA(bq[s % 6], A[0], (f32x16){});
        else aY = MFMA(bq[s % 6], A[18 * HALF + s - 18], aY);
      }
      if (START == 1) {
        if (s <= 14) { ALOAD(6 + 2 * s); ALOAD(7 + 2 * s); }
        if (NW == 8 && s >= 18 && s < 28) {
          const int cc = 2 + (s - 18) / 5, i = (s - 18) % 5;
          int ld2; const u16* src2 = chunk_src(cc, ld2);
          if (i < 4 || wid < 4) glds16(src2 + (size_t)i * NW * 8 * ld2, (unsigned)__builtin_amdgcn_readfirstlane(lds0 + cc * SLOT_B + wid * 1024 + i * NW * 1024));
        }
      }
      if (SIL != 0) {
        constexpr int B0 = (SIL == 1) ? 18 : 0;
        const f32x16& g = (SIL == 1) ? aX : sg;
        const int r1 = s - B0, r2 = s - B0 - 1, r3 = s - B0 - 2;
        if (r3 >= 0 && r3 < 16) {
          sv[r3 & 1] = g[r3] * e2[r3 & 1];
          if (r3 & 1) { unsigned w = pack2(sv[0], sv[1]); PIN(w); if (r3 < 8) s0[r3 >> 1] = w; else s1[(r3 - 8) >> 1] = w; }
        }
        if (r2 >= 0 && r2 < 16) { e2[r2 & 1] = __builtin_amdgcn_rcpf(1.0f + e1[r2 & 1]); PIN(e2[r2 & 1]); }
        if (r1 >= 0 && r1 < 16) { e1[r1 & 1] = __builtin_amdgcn_exp2f(-1.4426950408889634f * g[r1]); PIN(e1[r1 & 1]); }
      }
      SBAR();
    }
    ++c;
    STAMP(MODE, 1 + c);
  };

  auto run_tail = [&](f32x16& acc) __attribute__((always_inline)) {
    int so = __builtin_amdgcn_readfirstlane((c % NSLOT) * SLOT_B), sn = __builtin_amdgcn_readfirstlane(((c + 1) % NSLOT) * SLOT_B);
    asm volatile("" : "+s"(so), "+s"(sn));
    const lds_cptr rdO = rdA + ((tail & 1) ? dB : 0);
    const unsigned target = (unsigned)(NW * (c + 1));
    unsigned fl = 0;
#pragma unroll
    for (int s = 0; s < 36; ++s) {
      if (s == 1) {
        PIN(acc);
        WAITV(0);
        if (lane == 0) __hip_atomic_fetch_add((__attribute__((address_space(3))) unsigned*)((lds_ptr)lds + cnt_off), 1u, __ATOMIC_RELAXED, __HIP_MEMORY_SCOPE_WORKGROUP);
      }
      if (s == 9) fl = *cnt;
      if (s == 12) {
        PIN(acc);
        while ((unsigned)__builtin_amdgcn_readfirstlane(fl) < target) { __builtin_amdgcn_s_sleep(1); fl = *cnt; }
        asm volatile("" ::: "memory");
      }
      const int sp = s + 5;
      if (sp < 18) bq[sp % 6] = wfrag<OFF2>(rdO + so + sp * 2048);
      else if (sp < 36) bq[sp % 6] = wfrag<OFF2>(rdO + sn + (sp - 18) * 2048);
      if (s == 0) acc = MFMA(bq[0], A[0], (f32x16){});
      else acc = MFMA(bq[s % 6], A[s], acc);
      SBAR();
    }
  };

  f32x16 a0, a1;
  u32x4 sa0, sa1, sb0, sb1;
  typedef IntC<0> I0; typedef IntC<1> I1; typedef IntC<2> I2;
  if (MODE == 0) {
    run_chunk(a0, a1, I0{}, I0{}, I0{}, I1{}, a1, sa0, sa1, 0);
    run_chunk(a0, a1, I1{}, I1{}, I0{}, I2{}, a1, sa0, sa1, 0);
    for (int gi = 0; gi < ngu; ++gi) {
      run_chunk(a0, a1, I0{}, I2{}, I0{}, I0{}, a1, sb0, sb1, 0);
      run_chunk(a0, a1, I1{}, I0{}, I0{}, I0{}, a1, sb0, sb1, 0);
      epi_h(sa0, sa1, a0, 2 * (gu0 + gi));
      epi_h(sb0, sb1, a1, 2 * (gu0 + gi) + 1);
      if (gi + 1 < ngu) {
        run_chunk(a0, a1, I0{}, I0{}, IntC<4>{}, I0{}, a1, sa0, sa1, 0);
        run_chunk(a0, a1, I1{}, I1{}, I0{}, I0{}, a1, sa0, sa1, 0);
      }
    }
    for (int di = 0; di < ndt; ++di) {
      run_chunk(a0, a1, I0{}, I0{}, IntC<-1>{}, I0{}, a1, sa0, sa1, di ? 16 : 4);
      run_chunk(a0, a1, I1{}, I0{}, I0{}, I0{}, a1, sa0, sa1, 0);
      epi_single(a0, 2 * (dt0 + di));
      epi_single(a1, 2 * (dt0 + di) + 1);
    }
  } else {
    run_chunk(a0, a1, I0{}, I0{}, I0{}, I1{}, a1, sa0, sa1, 0);
    run_chunk(a0, a1, I1{}, I0{}, I0{}, I2{}, a1, sa0, sa1, 0);
    epi_single(a0, 2 * dt0);
    epi_single(a1, 2 * dt0 + 1);
    for (int di = 1; di < ndt; ++di) {
      run_chunk(a0, a1, I0{}, I0{}, IntC<8>{}, I0{}, a1, sa0, sa1, 0);
      run_chunk(a0, a1, I1{}, I0{}, I0{}, I0{}, a1, sa0, sa1, 0);
      epi_single(a0, 2 * (dt0 + di));
      epi_single(a1, 2 * (dt0 + di) + 1);
    }
    if (MODE == 1 && tail >= 0) {
      run_tail(a0);
      epi_single(a0, tail);
    }
  }
  STAMP(MODE, 20);
}

__global__ __launch_bounds__(512, 2) void k_gemm1(const u16* __restrict__ wqkv, const u16* __restrict__ w1,
                                                  const u16* __restrict__ w3, char* __restrict__ ws) {
  extern __shared__ __attribute__((aligned(16))) char lds[];
  const int b = blockIdx.x, xcd = b & 7, idx = b >> 3;
  const int panel = idx & 7, cg = xcd * 4 + (idx >> 3);
  int ngu, gu0, ndt, dt0;
  if (cg < 8) { ngu = 3; gu0 = 3 * cg; ndt = 0; dt0 = 0; }
  else { const int j = cg - 8; ngu = 2; gu0 = 24 + 2 * j; ndt = j < 3 ? 2 : 1; dt0 = j < 3 ? 2 * j : 3 + j; }
  const int rowblk = panel * 8 + (threadIdx.x >> 6);
  gemm_core<0, 8>(ws + WS_XN + (size_t)rowblk * 36 * 1024, ngu, gu0, ndt, dt0, wqkv, w1, w3, ws, nullptr, nullptr,
                  rowblk, 0, lds, NSLOT * SLOT_B, -1);
}

__global__ __launch_bounds__(512, 2) void k_down(const u16* __restrict__ w2, char* __restrict__ ws) {
  extern __shared__ __attribute__((aligned(16))) char lds[];
  const int b = blockIdx.x, ks = b & 7, idx = b >> 3;
  const int panel = idx & 7, nq = idx >> 3;
  const int ndt = 2, dt0 = 2 * nq, tail = nq < 2 ? 16 + nq : -1;
  const int rowblk = panel * 8 + (threadIdx.x >> 6);
  gemm_core<1, 8>(ws + WS_H + ((size_t)rowblk * 288 + ks * 36) * 1024, 0, 0, ndt, dt0, w2, nullptr, nullptr, ws, nullptr,
                  nullptr, rowblk, ks, lds, NSLOT * SLOT_B, tail);
}

__global__ __launch_bounds__(128, 2) void k_final(const u16* __restrict__ wo, char* __restrict__ ws,
                                                  const u16* __restrict__ x, u16* __restrict__ out) {
  extern __shared__ __attribute__((aligned(16))) char lds[];
  const int b = blockIdx.x, rp = b & 31, u = b >> 5;
  const int rowblk = rp * 2 + (threadIdx.x >> 6);
  gemm_core<2, 2>(ws + WS_O + (size_t)rowblk * 36 * 1024, 0, 0, 1, u, wo, nullptr, nullptr, ws, x, out, rowblk, 0, lds, 2 * SLOT_B, -1);
}

constexpr int ATTN_LDS = 163840;

__global__ __launch_bounds__(768, 3) void k_attn(char* __restrict__ ws) {
  extern __shared__ __attribute__((aligned(16))) char lds[];
  const int tid = threadIdx.x, lane = tid & 63, r32 = lane & 31, hi = lane >> 5;
  const int wid = __builtin_amdgcn_readfirstlane(tid >> 6);
  const int b = blockIdx.x, xcd = b & 7, idx = b >> 3;
  const int head = xcd * 4 + (idx >> 3), qb = idx & 7;
  const unsigned lds0 = (unsigned)(uintptr_t)lds;
  STAMP(3, 0);
  if (wid >= 8) {
    WAITV_BAR(0);
    WAIT_BAR(0);
    for (int pc = wid - 8; pc < 18; pc += 4) {
      const size_t off = ((size_t)b * 18 + pc) * 1024 + lane * 16;
      f32x4 v[8];
#pragma unroll
      for (int sl = 0; sl < 8; ++sl) v[sl] = __builtin_nontemporal_load((const f32x4*)(ws + WS_P + (size_t)sl * (64 * 18 * 4 * 1024) + off));
      f32x4 sum = ((v[0] + v[1]) + (v[2] + v[3])) + ((v[4] + v[5]) + (v[6] + v[7]));
      *(f32x4*)(ws + WS_PS + off) = sum;
    }
    return;
  }
  {
    const char* src = ws + WS_KV + (size_t)head * 2 * QH + wid * 1024 + lane * 16;
#pragma unroll
    for (int i = 0; i < 20; ++i) glds16(src + i * 8192, (unsigned)__builtin_amdgcn_readfirstlane(lds0 + wid * 1024 + i * 8192));
  }
  const int q = qb * 256 + wid * 32 + r32;
  const char* qp = ws + WS_Q + (size_t)head * QH + (size_t)q * 40;
  u32x2 qa = *(const u32x2*)(qp + 16 * hi), qb2 = *(const u32x2*)(qp + 16 * hi + 8);
  unsigned q1w = *(const unsigned*)(qp + 32);
  asm volatile("" : "+v"(qa), "+v"(qb2), "+v"(q1w));
  WAITV_BAR(0);
  if (hi) q1w = 0;
  const bf16x8 q0 = __builtin_bit_cast(bf16x8, (u32x4){qa[0], qa[1], qb2[0], qb2[1]});
  const bf16x8 q1 = __builtin_bit_cast(bf16x8, (u32x4){q1w, 0u, 0u, 0u});
#pragma unroll
  for (int i = 0; i < 4; ++i) *(__attribute__((address_space(3))) unsigned*)((lds_ptr)lds + QH + (size_t)(tid + 512 * i) * 40 + 36) = 0x00003f80u;
  WAIT_BAR(0);
  STAMP(3, 1);

  typedef __attribute__((address_space(3))) const u32x2* lds_u2;
  typedef __attribute__((address_space(3))) const unsigned* lds_u;
  const lds_cptr kp = (lds_cptr)lds + r32 * 40 + 16 * hi;
  const lds_cptr kp1 = (lds_cptr)lds + r32 * 40 + 32;
  const lds_cptr vp = (lds_cptr)lds + QH + (4 * hi + ((lane & 15) >> 2)) * 40 + (((lane >> 4) & 1) ? 32 : (lane & 3) * 8);

#define LOADK(t, kf0, kf1)                                                                   \
  {                                                                                          \
    u32x2 a_ = *(lds_u2)(kp + (t) * 1280), b_ = *(lds_u2)(kp + (t) * 1280 + 8);              \
    kf0 = __builtin_bit_cast(bf16x8, (u32x4){a_[0], a_[1], b_[0], b_[1]});                   \
    kf1 = __builtin_bit_cast(bf16x8, (u32x4){*(lds_u)(kp1 + (t) * 1280), 0u, 0u, 0u});       \
  }
#define LOADV(t, vf0, vf1)                                                                   \
  {                                                                                          \
    vf0 = wfrag<320>(vp + (t) * 1280);                                                       \
    vf1 = wfrag<320>(vp + (t) * 1280 + 640);                                                 \
  }
#define EX(x) __builtin_amdgcn_exp2f(x)

  f32x16 negm;
  bf16x8 kf0, kf1, vf0, vf1;
  {
    LOADK(0, kf0, kf1);
    f32x16 s = MFMA(kf0, q0, (f32x16){});
    s = MFMA(kf1, q1, s);
    float mx = s[0];
#pragma unroll
    for (int r = 1; r < 16; ++r) mx = fmaxf(mx, s[r]);
    mx = fmaxf(mx, __shfl_xor(mx, 32));
#pragma unroll
    for (int r = 0; r < 16; ++r) negm[r] = -mx;
  }
  f32x16 o = {};
  f32x16 sA, sB;
  sA = MFMA(kf0, q0, negm);
  sA = MFMA(kf1, q1, sA);
  LOADK(1, kf0, kf1);
  LOADV(0, vf0, vf1);
#define LOADK2(P0, P1, OFF)                                                                  \
  {                                                                                          \
    u32x2 a_ = *(lds_u2)((P0) + (OFF));                                                      \
    asm volatile("" ::: "memory");                                                           \
    u32x2 b_ = *(lds_u2)((P0) + (OFF) + 8);                                                  \
    kf0 = __builtin_bit_cast(bf16x8, (u32x4){a_[0], a_[1], b_[0], b_[1]});                   \
    kf1 = __builtin_bit_cast(bf16x8, (u32x4){*(lds_u)((P1) + (OFF)), 0u, 0u, 0u});           \
  }
#define STEP(SC, SN, KO, VO, NX, PK, PV)                                                                         \
  {                                                                                                              \
    u32x4 p0, p1;                                                                                                \
    if (NX) SN = MFMA(kf0, q0, negm);                                                                            \
    SC[0] = EX(SC[0]); SC[1] = EX(SC[1]); SC[2] = EX(SC[2]); SC[3] = EX(SC[3]);                                  \
    SC[4] = EX(SC[4]); SC[5] = EX(SC[5]);                                                                        \
    p0[0] = pack2(SC[0], SC[1]); p0[1] = pack2(SC[2], SC[3]); PIN(p0); SBAR();                                   \
    if (NX) SN = MFMA(kf1, q1, SN);                                                                              \
    if (PK) LOADK2(kq, kq1, KO);                                                                                 \
    SC[6] = EX(SC[6]); SC[7] = EX(SC[7]); SC[8] = EX(SC[8]); SC[9] = EX(SC[9]);                                  \
    p0[2] = pack2(SC[4], SC[5]); p0[3] = pack2(SC[6], SC[7]); PIN(p0); SBAR();                                   \
    SC[10] = EX(SC[10]); SC[11] = EX(SC[11]);                                                                    \
    o = MFMA(vf0, __builtin_bit_cast(bf16x8, p0), o);                                                            \
    SC[12] = EX(SC[12]); SC[13] = EX(SC[13]);                                                                    \
    p1[0] = pack2(SC[8], SC[9]); p1[1] = pack2(SC[10], SC[11]); PIN(p1); SBAR();                                 \
    SC[14] = EX(SC[14]); SC[15] = EX(SC[15]);                                                                    \
    p1[2] = pack2(SC[12], SC[13]); PIN(p1); SBAR();                                                              \
    p1[3] = pack2(SC[14], SC[15]); PIN(p1); SBAR();                                                              \
    o = MFMA(vf1, __builtin_bit_cast(bf16x8, p1), o);                                                            \
    if (PV) { vf0 = wfrag<320>(vq + (VO)); vf1 = wfrag<320>(vq + (VO) + 640); }                                  \
    SBAR();                                                                                                      \
  }
  {
    lds_cptr kq = kp + 2560, kq1 = kp1 + 2560, vq = vp + 1280;
    for (int t = 0; t < 62; t += 2) {
      PIN(vq); PIN(kq); PIN(kq1);
      STEP(sA, sB, 0, 0, true, true, true);
      STEP(sB, sA, 1280, 1280, true, true, true);
      kq += 2560; kq1 += 2560; vq += 2560;
    }
    STEP(sA, sB, 0, 0, true, false, true);
    STEP(sB, sA, 0, 0, false, false, false);
  }
  STAMP(3, 2);
  float l = __shfl(o[10], r32);
  if (__any(!(l < 1.0e30f))) {
    float mrun = -INFINITY;
    o = (f32x16){};
    for (int t = 0; t < 64; ++t) {
      LOADK(t, kf0, kf1);
      LOADV(t, vf0, vf1);
      f32x16 s = MFMA(kf0, q0, (f32x16){});
      s = MFMA(kf1, q1, s);
      float mx = s[0];
#pragma unroll
      for (int r = 1; r < 16; ++r) mx = fmaxf(mx, s[r]);
      mx = fmaxf(mx, __shfl_xor(mx, 32));
      const float mnew = fmaxf(mrun, mx);
      const float alpha = __builtin_amdgcn_exp2f(mrun - mnew);
      mrun = mnew;
#pragma unroll
      for (int r = 0; r < 16; ++r) o[r] *= alpha;
      u32x4 p0, p1;
#pragma unroll
      for (int j = 0; j < 4; ++j) {
        p0[j] = pack2(__builtin_amdgcn_exp2f(s[2 * j] - mnew), __builtin_amdgcn_exp2f(s[2 * j + 1] - mnew));
        p1[j] = pack2(__builtin_amdgcn_exp2f(s[8 + 2 * j] - mnew), __builtin_amdgcn_exp2f(s[9 + 2 * j] - mnew));
      }
      o = MFMA(vf0, __builtin_bit_cast(bf16x8, p0), o);
      o = MFMA(vf1, __builtin_bit_cast(bf16x8, p1), o);
    }
    l = __shfl(o[10], r32);
  }
  const float rl = 1.0f / l;
  char* ob = ws + WS_O + ((size_t)(q >> 5) * 36 * 64 + (q & 31)) * 16;
#pragma unroll
  for (int i = 0; i < 5; ++i) {
    const int d = (i >> 1) * 8 + 4 * hi + (i & 1) * 2;
    const int r = (i >> 1) * 4 + (i & 1) * 2;
    const int f = head * DH_ + d;
    if (i < 4 || !hi) *(unsigned*)(ob + ((f >> 4) * 64 + ((f >> 3) & 1) * 32) * 16 + (f & 7) * 2) = pack2(o[r] * rl, o[r + 1] * rl);
  }
  STAMP(3, 20);
}

extern "C" void kernel_launch(void* const* d_in, const int* in_sizes, int n_in, void* d_out, int out_size,
                              void* d_ws, size_t ws_size, hipStream_t stream) {
  const u16* x = (const u16*)d_in[0];
  const u16* wqkv = (const u16*)d_in[1];
  const u16* wo = (const u16*)d_in[2];
  const u16* w1 = (const u16*)d_in[4];
  const u16* w3 = (const u16*)d_in[5];
  const u16* w2 = (const u16*)d_in[6];
  char* ws = (char*)d_ws;
  u16* out = (u16*)d_out;
  (void)hipFuncSetAttribute((const void*)k_gemm1, hipFuncAttributeMaxDynamicSharedMemorySize, GEMM_LDS);
  (void)hipFuncSetAttribute((const void*)k_down, hipFuncAttributeMaxDynamicSharedMemorySize, GEMM_LDS);
  (void)hipFuncSetAttribute((const void*)k_final, hipFuncAttributeMaxDynamicSharedMemorySize, GEMM_LDS);
  (void)hipFuncSetAttribute((const void*)k_attn, hipFuncAttributeMaxDynamicSharedMemorySize, ATTN_LDS);
  k_ln<<<256, 64, 0, stream>>>(x, ws);
  k_gemm1<<<256, 512, GEMM_LDS, stream>>>(wqkv, w1, w3, ws);
  k_down<<<256, 512, GEMM_LDS, stream>>>(w2, ws);
  k_attn<<<256, 768, ATTN_LDS, stream>>>(ws);
  k_final<<<288, 128, 2 * SLOT_B + 16, stream>>>(wo, ws, x, out);
}
```

```cpp
#include <hip/hip_runtime.h>
#include <hip/hip_bf16.h>
#include <stdint.h>

typedef unsigned short u16;
typedef short bf16x8 __attribute__((ext_vector_type(8)));
typedef short s16x4 __attribute__((ext_vector_type(4)));
typedef float f32x16 __attribute__((ext_vector_type(16)));
typedef float f32x4 __attribute__((ext_vector_type(4)));
typedef float f32x2 __attribute__((ext_vector_type(2)));
typedef unsigned u32x4 __attribute__((ext_vector_type(4)));
typedef unsigned u32x2 __attribute__((ext_vector_type(2)));
typedef __bf16 bf16x2_t __attribute__((ext_vector_type(2)));
typedef short v4i16_t __attribute__((ext_vector_type(4)));
typedef __attribute__((address_space(3))) const char* lds_cptr;
typedef __attribute__((address_space(3))) char* lds_ptr;

#define DI __device__ __forceinline__
#define MFMA(a, b, c) __builtin_amdgcn_mfma_f32_32x32x16_bf16((a), (b), (c), 0, 0, 0)
#define SBAR() __builtin_amdgcn_sched_barrier(0)

constexpr int S_ = 2048, D_ = 576, H_ = 32, DH_ = 18, F_ = 4608;
constexpr size_t WS_XN = 0;
constexpr size_t WS_Q = WS_XN + (size_t)S_ * D_ * 2;
constexpr size_t QH = 81920;
constexpr size_t WS_KV = WS_Q + (size_t)H_ * QH;
constexpr size_t WS_H = WS_KV + (size_t)H_ * 2 * QH;
constexpr size_t WS_O = WS_H + (size_t)S_ * F_ * 2;
constexpr size_t WS_P = WS_O + (size_t)S_ * D_ * 2;
constexpr size_t WS_PS = WS_P + (size_t)8 * S_ * D_ * 4;
constexpr size_t WS_END = WS_PS + (size_t)S_ * D_ * 4;

#define STAMP(kid, i) do {} while (0)
DI unsigned pack2(float a, float b) {
  bf16x2_t r = __builtin_convertvector((f32x2){a, b}, bf16x2_t);
  return __builtin_bit_cast(unsigned, r);
}
DI void st16_wt(char* ws, size_t off, u32x4 v) {
  __builtin_amdgcn_raw_buffer_store_b128(v, __builtin_amdgcn_make_buffer_rsrc(ws, 0, 0x7fffffff, 0x00020000), (int)off, 0, 17);
}
DI float bflo(unsigned w) { return __uint_as_float(w << 16); }
DI float bfhi(unsigned w) { return __uint_as_float(w & 0xffff0000u); }

DI void glds16(const void* g, unsigned lds_base) {
  unsigned sv;
  asm volatile("s_mov_b32 %0, m0\n\ts_mov_b32 m0, %2\n\ts_nop 0\n\tglobal_load_lds_dwordx4 %1, off\n\ts_mov_b32 m0, %0"
               : "=&s"(sv) : "v"(g), "s"(lds_base) : "memory");
}
DI s16x4 vtr(lds_cptr p) {
  return __builtin_bit_cast(s16x4, __builtin_amdgcn_ds_read_tr16_b64_v4i16((__attribute__((address_space(3))) v4i16_t*)p));
}
template <int OFF2>
DI bf16x8 wfrag(lds_cptr p) {
  s16x4 lo = vtr(p), hi = vtr(p + OFF2);
  return (bf16x8){lo[0], lo[1], lo[2], lo[3], hi[0], hi[1], hi[2], hi[3]};
}
#define WAITV_BAR(N) asm volatile("s_waitcnt vmcnt(" #N ")\n\ts_barrier" ::: "memory")
#define WAITV(N) asm volatile("s_waitcnt vmcnt(" #N ")" ::: "memory")
#define WAITVN(N) asm volatile("s_waitcnt vmcnt(%0)" :: "i"(N) : "memory")
#define WAIT_BAR(N) asm volatile("s_waitcnt vmcnt(" #N ") lgkmcnt(0)\n\ts_barrier" ::: "memory")

__global__ __launch_bounds__(64) void k_ln(const u16* __restrict__ x, char* __restrict__ ws) {
  const int lane = threadIdx.x & 63, wid = threadIdx.x >> 6;
  const int row = (blockIdx.x + wid) * 8 + (lane >> 3), c8 = lane & 7;
  const u16* xr = x + (size_t)row * D_ + c8 * 8;
  u32x4 a[9];
#pragma unroll
  for (int j = 0; j < 9; ++j) a[j] = *(const u32x4*)(xr + 64 * j);
  float s = 0.f;
#pragma unroll
  for (int j = 0; j < 9; ++j)
#pragma unroll
    for (int i = 0; i < 4; ++i) s += bflo(a[j][i]) + bfhi(a[j][i]);
  s += __shfl_xor(s, 1); s += __shfl_xor(s, 2); s += __shfl_xor(s, 4);
  const float mean = s * (1.0f / D_);
  float q = 0.f;
#pragma unroll
  for (int j = 0; j < 9; ++j)
#pragma unroll
    for (int i = 0; i < 4; ++i) { float d0 = bflo(a[j][i]) - mean, d1 = bfhi(a[j][i]) - mean; q += d0 * d0 + d1 * d1; }
  q += __shfl_xor(q, 1); q += __shfl_xor(q, 2); q += __shfl_xor(q, 4);
  const float rstd = 1.0f / sqrtf(q * (1.0f / D_) + 1e-5f);
  char* xn = ws + WS_XN + ((size_t)(row >> 5) * 36 * 64 + (row & 31)) * 16;
#pragma unroll
  for (int j = 0; j < 9; ++j) {
    u32x4 o;
#pragma unroll
    for (int i = 0; i < 4; ++i) o[i] = pack2((bflo(a[j][i]) - mean) * rstd, (bfhi(a[j][i]) - mean) * rstd);
    const int c = c8 + 8 * j;
    *(u32x4*)(xn + ((c >> 1) * 64 + (c & 1) * 32) * 16) = o;
  }
}

constexpr int NSLOT = 4, SLOT_B = 36864;
constexpr int GEMM_LDS = NSLOT * SLOT_B + 16 + 8192;

template <int V> struct IntC { static constexpr int value = V; };
#define PIN(x) asm volatile("" : "+v"(x))

template <int MODE, int NW>
DI void gemm_core(const char* __restrict__ Af  ,
                  int ngu, int gu0, int ndt, int dt0,
                  const u16* __restrict__ W0, const u16* __restrict__ W1, const u16* __restrict__ W2,
                  char* __restrict__ ws, const u16* __restrict__ xin, u16* __restrict__ out,
                  int rowblk  , int kslice, char* lds, int cnt_off, int tail) {
  const int tid = threadIdx.x, lane = tid & 63, r32 = lane & 31, hi = lane >> 5;
  const int wid = __builtin_amdgcn_readfirstlane(tid >> 6);
  const unsigned lds0 = (unsigned)(uintptr_t)lds;
  const int nchunk = 4 * ngu + 2 * ndt + (tail >= 0 ? 2 : 0);
  STAMP(MODE, 0);
  typedef __attribute__((address_space(3))) volatile unsigned* lds_vu;
  const lds_vu cnt = (lds_vu)((lds_ptr)lds + cnt_off);
  if (tid == 0) *cnt = 0;

  auto chunk_src = [&](int c, int& ld) __attribute__((always_inline)) -> const u16* {
    const u16* base;
    if (MODE == 0) {
      if (c < 4 * ngu) { base = ((c & 2) ? W2 : W1) + 64 * (gu0 + (c >> 2)); ld = F_; }
      else { base = W0 + 64 * (dt0 + ((c - 4 * ngu) >> 1)); ld = 3 * D_; }
    } else if (MODE == 1) {
      base = W0 + (size_t)kslice * 576 * D_ + 64 * ((c >> 1) < ndt ? dt0 + (c >> 1) : (tail >> 1)); ld = D_;
    } else {
      base = W0 + 64 * (dt0 + (c >> 1)); ld = D_;
    }
    const int row = wid * 8 + (lane >> 3);
    return base + (size_t)((c & 1) * 288 + row) * ld + ((lane & 7) ^ (((lane >> 4) & 1) << 2)) * 8;
  };
  constexpr int NPIECE = (36 + NW - 1) / NW;
  auto issue = [&](int c) __attribute__((always_inline)) {
    int ld; const u16* src = chunk_src(c, ld);
    const unsigned dst = lds0 + (c % NSLOT) * SLOT_B + wid * 1024;
#pragma unroll
    for (int i = 0; i < NPIECE; ++i)
      if (i < 36 / NW || wid < 36 % NW)
        glds16(src + (size_t)i * NW * 8 * ld, (unsigned)__builtin_amdgcn_readfirstlane(dst + i * NW * 1024));
  };
  u32x2 pfx[8]; f32x4 pfm[8];
  if (MODE == 2) {
#pragma unroll
    for (int i = 0; i < 8; ++i) {
      const int t = 2 * dt0 + (i >> 2), g = i & 3;
      pfx[i] = *(const u32x2*)(xin + (size_t)(rowblk * 32 + r32) * D_ + 32 * t + 8 * g + 4 * hi);
      pfm[i] = *(const f32x4*)(ws + WS_PS + ((((size_t)rowblk * 18 + t) * 4 + g) * 64 + lane) * 16);
    }
  }
  issue(0);
  issue(1);
  bf16x8 A[36];
  const char* ap = Af + lane * 16;
#define ALOAD(k) asm volatile("global_load_dwordx4 %0, %1, off offset:%2" : "=v"(A[k]) : "v"(ap + ((k) >> 2) * 4096), "i"(((k) & 3) * 1024) : "memory")
#pragma unroll
  for (int s = 0; s < 6; ++s) ALOAD(s);
  asm volatile("s_waitcnt vmcnt(%0) lgkmcnt(0)\n\ts_barrier" :: "i"(6 + (NW == 8 ? 4 : 18)) : "memory");
  SBAR();
  STAMP(MODE, 1);

  constexpr int OFF2 = (MODE == 1) ? 1024 : 512;
  const int q4 = (lane & 15) >> 2;
  const int rdo = ((MODE == 1 ? hi * 4 : hi * 8) + q4) * 128 + ((q4 >> 1) << 6) + ((lane >> 4) & 1) * 32 + (lane & 3) * 8;
  const lds_cptr rdA = (lds_cptr)lds + rdo, rdB = (lds_cptr)lds + (rdo ^ 64);
  const int dB = (rdo ^ 64) - rdo;
  const int m = rowblk * 32 + r32;

  auto silu = [&](float g) __attribute__((always_inline)) -> float { return g * __builtin_amdgcn_rcpf(1.0f + __builtin_amdgcn_exp2f(-1.4426950408889634f * g)); };
  auto epi_h = [&](const u32x4& sa, const u32x4& sb, const f32x16& up, int t32) __attribute__((always_inline)) {
    const size_t off = WS_H + (((size_t)rowblk * 288 + 2 * t32) * 64 + lane) * 16;
    u32x4 w0, w1;
#pragma unroll
    for (int j = 0; j < 4; ++j) {
      w0[j] = pack2(bflo(sa[j]) * up[2 * j], bfhi(sa[j]) * up[2 * j + 1]);
      w1[j] = pack2(bflo(sb[j]) * up[8 + 2 * j], bfhi(sb[j]) * up[9 + 2 * j]);
    }
    st16_wt(ws, off, w0);
    st16_wt(ws, off + 1024, w1);
  };
  auto epi_single = [&](const f32x16& a, int t) __attribute__((always_inline)) {
    if (MODE == 0) {
      const lds_ptr stg = (lds_ptr)lds + NSLOT * SLOT_B + 16 + wid * 1024;
      const int n = 32 * t + 2 * (lane & 15);
      const int which = n / D_, f = n - which * D_, head = f / DH_, d = f - head * DH_;
      const size_t off = (which == 0 ? (WS_Q + (size_t)head * QH) : (WS_KV + (size_t)head * 2 * QH + (size_t)(which - 1) * QH)) +
                         (size_t)(rowblk * 32 + (lane >> 4)) * 40 + d * 2;
      const float sc = (32 * t < D_) ? (0.23570226039551584f * 1.4426950408889634f) : 1.0f;
#pragma unroll
      for (int rho = 0; rho < 2; ++rho) {
        if ((r32 >> 4) == rho) {
#pragma unroll
          for (int g = 0; g < 4; ++g)
            *(__attribute__((address_space(3))) u32x2*)(stg + (r32 & 15) * 64 + (8 * g + 4 * hi) * 2) =
                (u32x2){pack2(a[4 * g] * sc, a[4 * g + 1] * sc), pack2(a[4 * g + 2] * sc, a[4 * g + 3] * sc)};
        }
        asm volatile("s_waitcnt lgkmcnt(0)" ::: "memory");
        unsigned wv[4];
#pragma unroll
        for (int i = 0; i < 4; ++i) wv[i] = *(__attribute__((address_space(3))) const unsigned*)(stg + ((lane >> 4) + 4 * i) * 64 + (lane & 15) * 4);
#pragma unroll
        for (int i = 0; i < 4; ++i) *(unsigned*)(ws + off + (size_t)(16 * rho + 4 * i) * 40) = wv[i];
        asm volatile("s_waitcnt lgkmcnt(0)" ::: "memory");
      }
    } else if (MODE == 1) {
      const size_t off = WS_P + (((((size_t)kslice * 64 + rowblk) * 18 + t) * 4) * 64 + lane) * 16;
#pragma unroll
      for (int g = 0; g < 4; ++g)
        st16_wt(ws, off + g * 1024, __builtin_bit_cast(u32x4, (f32x4){a[4 * g], a[4 * g + 1], a[4 * g + 2], a[4 * g + 3]}));
    } else {
#pragma unroll
      for (int g = 0; g < 4; ++g) {
        const int col = 32 * t + 8 * g + 4 * hi;
        const u32x2 xv = pfx[(t & 1) * 4 + g];
        const f32x4 pm = pfm[(t & 1) * 4 + g];
        f32x4 sum = {a[4 * g] + pm[0] + bflo(xv[0]), a[4 * g + 1] + pm[1] + bfhi(xv[0]),
                     a[4 * g + 2] + pm[2] + bflo(xv[1]), a[4 * g + 3] + pm[3] + bfhi(xv[1])};
        *(u32x2*)(out + (size_t)m * D_ + col) = (u32x2){pack2(sum[0], sum[1]), pack2(sum[2], sum[3])};
      }
    }
  };

  bf16x8 bq[6];
#pragma unroll
  for (int s = 0; s < 5; ++s) bq[s] = wfrag<OFF2>(rdA + s * 2048);

  int c = 0;
  auto run_chunk = [&](f32x16& aX, f32x16& aY, auto half_, auto sil_, auto nst_, auto start_, f32x16& sg, u32x4& s0, u32x4& s1, int nrt) __attribute__((always_inline)) {
    constexpr int HALF = decltype(half_)::value, SIL = decltype(sil_)::value, NST = decltype(nst_)::value, START = decltype(start_)::value;
    int so = __builtin_amdgcn_readfirstlane((c % NSLOT) * SLOT_B);
    const int cn = c + 1 < nchunk ? c + 1 : c;
    int sn = __builtin_amdgcn_readfirstlane((cn % NSLOT) * SLOT_B);
    asm volatile("" : "+s"(so), "+s"(sn));
    const lds_cptr pn = rdA + sn + ((MODE == 1 && tail >= 0 && c + 3 == nchunk && (tail & 1)) ? dB : 0);
    const bool doiss = START != 1 && c + 3 < nchunk;
    int nld; const u16* nsrc = chunk_src(doiss ? c + 3 : c, nld);
    const size_t nstr = (size_t)NW * 8 * nld;
    const unsigned ndst = (unsigned)__builtin_amdgcn_readfirstlane(lds0 + ((c + 3) % NSLOT) * SLOT_B + wid * 1024);
    const unsigned target = (unsigned)(NW * (c + 1));
    unsigned fl = 0;
    float sv[2], e1[2], e2[2];
#pragma unroll
    for (int s = 0; s < 36; ++s) {
      if (NW == 8 && s == 0) __builtin_amdgcn_s_setprio(2);
      if (NW == 8 && s == 18) __builtin_amdgcn_s_setprio(0);
      if (NW == 8 && s == (START == 2 ? 20 : 1)) {
        PIN(aX); PIN(aY);
        if (START == 2) {
          if (c + 2 < nchunk) { WAITV(4); } else { WAITV(0); }
        } else if (START == 0) {
          if (c + 2 < nchunk) {
            if (NST >= 0) { if (wid < 4) { WAITVN(5 + (NST >= 0 ? NST : 0)); } else { WAITVN(4 + (NST >= 0 ? NST : 0)); } }
            else if (nrt == 16) { if (wid < 4) { WAITVN(21); } else { WAITVN(20); } }
            else { if (wid < 4) { WAITVN(9); } else { WAITVN(8); } }
          } else { WAITV(0); }
        }
        if (lane == 0) __hip_atomic_fetch_add((__attribute__((address_space(3))) unsigned*)((lds_ptr)lds + cnt_off), 1u, __ATOMIC_RELAXED, __HIP_MEMORY_SCOPE_WORKGROUP);
      }
      if (NW != 8 && START == 1 && s == 26) { PIN(aX); PIN(aY); asm volatile("s_barrier" ::: "memory"); }
      if (NW == 8 && s == 23) fl = *cnt;
      if (NW == 8 && s == 26) {
        PIN(aX); PIN(aY);
        while ((unsigned)__builtin_amdgcn_readfirstlane(fl) < target) { __builtin_amdgcn_s_sleep(1); fl = *cnt; }
        asm volatile("" ::: "memory");
      }
      if (NW == 8 && s >= 27 && (s - 27) % 2 == 0) {
        const int i = (s - 27) / 2;
        PIN(aY);
        if (doiss && (i < 4 || wid < 4)) glds16(nsrc + i * nstr, ndst + i * NW * 1024);
      }
      {
        const int sp = s + 5;
        if (sp < 18) bq[sp % 6] = wfrag<OFF2>(rdA + so + sp * 2048);
        else if (sp < 36) bq[sp % 6] = wfrag<OFF2>(rdB + so + (sp - 18) * 2048);
        else bq[sp % 6] = wfrag<OFF2>(pn + (sp - 36) * 2048);
      }
      if (START == 1 && s < 18) asm volatile("s_waitcnt vmcnt(%1)" : "+v"(A[s]) : "i"(s <= 15 ? 5 + s : 35 - s));
      if (START == 2 && s < 18) asm volatile("s_waitcnt vmcnt(%1)" : "+v"(A[18 + s]) : "i"(17 - s + (NW == 8 ? 8 : 0)));
      if (s < 18) {
        if (HALF == 0 && s == 0) aX = MFMA(bq[0], A[0], (f32x16){});
        else aX = MFMA(bq[s % 6], A[18 * HALF + s], aX);
      } else {
        if (HALF == 0 && s == 18) aY = MFMA(bq[s % 6], A[0], (f32x16){});
        else aY = MFMA(bq[s % 6], A[18 * HALF + s - 18], aY);
      }
      if (START == 1) {
        if (s <= 14) { ALOAD(6 + 2 * s); ALOAD(7 + 2 * s); }
        if (NW == 8 && s >= 18 && s < 28) {
          const int cc = 2 + (s - 18) / 5, i = (s - 18) % 5;
          int ld2; const u16* src2 = chunk_src(cc, ld2);
          if (i < 4 || wid < 4) glds16(src2 + (size_t)i * NW * 8 * ld2, (unsigned)__builtin_amdgcn_readfirstlane(lds0 + cc * SLOT_B + wid * 1024 + i * NW * 1024));
        }
      }
      if (SIL != 0) {
        constexpr int B0 = (SIL == 1) ? 18 : 0;
        const f32x16& g = (SIL == 1) ? aX : sg;
        const int r1 = s - B0, r2 = s - B0 - 1, r3 = s - B0 - 2;
        if (r3 >= 0 && r3 < 16) {
          sv[r3 & 1] = g[r3] * e2[r3 & 1];
          if (r3 & 1) { unsigned w = pack2(sv[0], sv[1]); PIN(w); if (r3 < 8) s0[r3 >> 1] = w; else s1[(r3 - 8) >> 1] = w; }
        }
        if (r2 >= 0 && r2 < 16) { e2[r2 & 1] = __builtin_amdgcn_rcpf(1.0f + e1[r2 & 1]); PIN(e2[r2 & 1]); }
        if (r1 >= 0 && r1 < 16) { e1[r1 & 1] = __builtin_amdgcn_exp2f(-1.4426950408889634f * g[r1]); PIN(e1[r1 & 1]); }
      }
      SBAR();
    }
    ++c;
    STAMP(MODE, 1 + c);
  };

  auto run_tail = [&](f32x16& acc) __attribute__((always_inline)) {
    int so = __builtin_amdgcn_readfirstlane((c % NSLOT) * SLOT_B), sn = __builtin_amdgcn_readfirstlane(((c + 1) % NSLOT) * SLOT_B);
    asm volatile("" : "+s"(so), "+s"(sn));
    const lds_cptr rdO = rdA + ((tail & 1) ? dB : 0);
    const unsigned target = (unsigned)(NW * (c + 1));
    unsigned fl = 0;
#pragma unroll
    for (int s = 0; s < 36; ++s) {
      if (s == 1) {
        PIN(acc);
        WAITV(0);
        if (lane == 0) __hip_atomic_fetch_add((__attribute__((address_space(3))) unsigned*)((lds_ptr)lds + cnt_off), 1u, __ATOMIC_RELAXED, __HIP_MEMORY_SCOPE_WORKGROUP);
      }
      if (s == 9) fl = *cnt;
      if (s == 12) {
        PIN(acc);
        while ((unsigned)__builtin_amdgcn_readfirstlane(fl) < target) { __builtin_amdgcn_s_sleep(1); fl = *cnt; }
        asm volatile("" ::: "memory");
      }
      const int sp = s + 5;
      if (sp < 18) bq[sp % 6] = wfrag<OFF2>(rdO + so + sp * 2048);
      else if (sp < 36) bq[sp % 6] = wfrag<OFF2>(rdO + sn + (sp - 18) * 2048);
      if (s == 0) acc = MFMA(bq[0], A[0], (f32x16){});
      else acc = MFMA(bq[s % 6], A[s], acc);
      SBAR();
    }
  };

  f32x16 a0, a1;
  u32x4 sa0, sa1, sb0, sb1;
  typedef IntC<0> I0; typedef IntC<1> I1; typedef IntC<2> I2;
  if (MODE == 0) {
    run_chunk(a0, a1, I0{}, I0{}, I0{}, I1{}, a1, sa0, sa1, 0);
    run_chunk(a0, a1, I1{}, I1{}, I0{}, I2{}, a1, sa0, sa1, 0);
    for (int gi = 0; gi < ngu; ++gi) {
      run_chunk(a0, a1, I0{}, I2{}, I0{}, I0{}, a1, sb0, sb1, 0);
      run_chunk(a0, a1, I1{}, I0{}, I0{}, I0{}, a1, sb0, sb1, 0);
      epi_h(sa0, sa1, a0, 2 * (gu0 + gi));
      epi_h(sb0, sb1, a1, 2 * (gu0 + gi) + 1);
      if (gi + 1 < ngu) {
        run_chunk(a0, a1, I0{}, I0{}, IntC<4>{}, I0{}, a1, sa0, sa1, 0);
        run_chunk(a0, a1, I1{}, I1{}, I0{}, I0{}, a1, sa0, sa1, 0);
      }
    }
    for (int di = 0; di < ndt; ++di) {
      run_chunk(a0, a1, I0{}, I0{}, IntC<-1>{}, I0{}, a1, sa0, sa1, di ? 16 : 4);
      run_chunk(a0, a1, I1{}, I0{}, I0{}, I0{}, a1, sa0, sa1, 0);
      epi_single(a0, 2 * (dt0 + di));
      epi_single(a1, 2 * (dt0 + di) + 1);
    }
  } else {
    run_chunk(a0, a1, I0{}, I0{}, I0{}, I1{}, a1, sa0, sa1, 0);
    run_chunk(a0, a1, I1{}, I0{}, I0{}, I2{}, a1, sa0, sa1, 0);
    epi_single(a0, 2 * dt0);
    epi_single(a1, 2 * dt0 + 1);
    for (int di = 1; di < ndt; ++di) {
      run_chunk(a0, a1, I0{}, I0{}, IntC<8>{}, I0{}, a1, sa0, sa1, 0);
      run_chunk(a0, a1, I1{}, I0{}, I0{}, I0{}, a1, sa0, sa1, 0);
      epi_single(a0, 2 * (dt0 + di));
      epi_single(a1, 2 * (dt0 + di) + 1);
    }
    if (MODE == 1 && tail >= 0) {
      run_tail(a0);
      epi_single(a0, tail);
    }
  }
  STAMP(MODE, 20);
}

__global__ __launch_bounds__(512, 2) void k_gemm1(const u16* __restrict__ wqkv, const u16* __restrict__ w1,
                                                  const u16* __restrict__ w3, char* __restrict__ ws,
                                                  const char* __restrict__ w2b, const char* __restrict__ wob) {
  extern __shared__ __attribute__((aligned(16))) char lds[];
  const int b = blockIdx.x, xcd = b & 7, idx = b >> 3;
  const int panel = idx & 7, cg = xcd * 4 + (idx >> 3);
  int ngu, gu0, ndt, dt0;
  if (cg < 11) { ngu = 3; gu0 = 3 * cg; ndt = 0; dt0 = 0; }
  else if (cg < 29) { const int j = cg - 11; ngu = 2; gu0 = 33 + 2 * j; ndt = 1; dt0 = j; }
  else { const int j = cg - 29; ngu = 1; gu0 = 69 + j; ndt = 3; dt0 = 18 + 3 * j; }
  const int rowblk = panel * 8 + (threadIdx.x >> 6);
  gemm_core<0, 8>(ws + WS_XN + (size_t)rowblk * 36 * 1024, ngu, gu0, ndt, dt0, wqkv, w1, w3, ws, nullptr, nullptr,
                  rowblk, 0, lds, NSLOT * SLOT_B, -1);
  if (cg >= 11 && cg < 29) {
    const int w = ((cg - 11) * 8 + panel) * 8 + (threadIdx.x >> 6);
    const int lane = threadIdx.x & 63;
    u32x4 acc = {0, 0, 0, 0};
#pragma unroll
    for (int i = 0; i < 6; ++i) {
      const int pc = w + 1152 * i;
      if (pc < 5184) acc ^= *(const u32x4*)(w2b + (size_t)pc * 1024 + lane * 16);
      else if (pc < 5832) acc ^= *(const u32x4*)(wob + (size_t)(pc - 5184) * 1024 + lane * 16);
    }
    asm volatile("" :: "v"(acc));
  }
}

__global__ __launch_bounds__(512, 2) void k_down(const u16* __restrict__ w2, char* __restrict__ ws) {
  extern __shared__ __attribute__((aligned(16))) char lds[];
  const int b = blockIdx.x, ks = b & 7, idx = b >> 3;
  const int panel = idx & 7, nq = idx >> 3;
  const int ndt = 2, dt0 = 2 * nq, tail = nq < 2 ? 16 + nq : -1;
  const int rowblk = panel * 8 + (threadIdx.x >> 6);
  gemm_core<1, 8>(ws + WS_H + ((size_t)rowblk * 288 + ks * 36) * 1024, 0, 0, ndt, dt0, w2, nullptr, nullptr, ws, nullptr,
                  nullptr, rowblk, ks, lds, NSLOT * SLOT_B, tail);
}

__global__ __launch_bounds__(128, 2) void k_final(const u16* __restrict__ wo, char* __restrict__ ws,
                                                  const u16* __restrict__ x, u16* __restrict__ out) {
  extern __shared__ __attribute__((aligned(16))) char lds[];
  const int b = blockIdx.x, rp = b & 31, u = b >> 5;
  const int rowblk = rp * 2 + (threadIdx.x >> 6);
  gemm_core<2, 2>(ws + WS_O + (size_t)rowblk * 36 * 1024, 0, 0, 1, u, wo, nullptr, nullptr, ws, x, out, rowblk, 0, lds, 2 * SLOT_B, -1);
}

constexpr int ATTN_LDS = 163840;

__global__ __launch_bounds__(768, 3) void k_attn(char* __restrict__ ws) {
  extern __shared__ __attribute__((aligned(16))) char lds[];
  const int tid = threadIdx.x, lane = tid & 63, r32 = lane & 31, hi = lane >> 5;
  const int wid = __builtin_amdgcn_readfirstlane(tid >> 6);
  const int b = blockIdx.x, xcd = b & 7, idx = b >> 3;
  const int head = xcd * 4 + (idx >> 3), qb = idx & 7;
  const unsigned lds0 = (unsigned)(uintptr_t)lds;
  STAMP(3, 0);
  if (wid >= 8) {
    WAITV_BAR(0);
    WAIT_BAR(0);
    for (int pc = wid - 8; pc < 18; pc += 4) {
      const size_t off = ((size_t)b * 18 + pc) * 1024 + lane * 16;
      f32x4 v[8];
#pragma unroll
      for (int sl = 0; sl < 8; ++sl) v[sl] = __builtin_nontemporal_load((const f32x4*)(ws + WS_P + (size_t)sl * (64 * 18 * 4 * 1024) + off));
      f32x4 sum = ((v[0] + v[1]) + (v[2] + v[3])) + ((v[4] + v[5]) + (v[6] + v[7]));
      *(f32x4*)(ws + WS_PS + off) = sum;
    }
    return;
  }
  {
    const char* src = ws + WS_KV + (size_t)head * 2 * QH + wid * 1024 + lane * 16;
#pragma unroll
    for (int i = 0; i < 20; ++i) glds16(src + i * 8192, (unsigned)__builtin_amdgcn_readfirstlane(lds0 + wid * 1024 + i * 8192));
  }
  const int q = qb * 256 + wid * 32 + r32;
  const char* qp = ws + WS_Q + (size_t)head * QH + (size_t)q * 40;
  u32x2 qa = *(const u32x2*)(qp + 16 * hi), qb2 = *(const u32x2*)(qp + 16 * hi + 8);
  unsigned q1w = *(const unsigned*)(qp + 32);
  asm volatile("" : "+v"(qa), "+v"(qb2), "+v"(q1w));
  WAITV_BAR(0);
  if (hi) q1w = 0;
  const bf16x8 q0 = __builtin_bit_cast(bf16x8, (u32x4){qa[0], qa[1], qb2[0], qb2[1]});
  const bf16x8 q1 = __builtin_bit_cast(bf16x8, (u32x4){q1w, 0u, 0u, 0u});
#pragma unroll
  for (int i = 0; i < 4; ++i) *(__attribute__((address_space(3))) unsigned*)((lds_ptr)lds + QH + (size_t)(tid + 512 * i) * 40 + 36) = 0x00003f80u;
  WAIT_BAR(0);
  STAMP(3, 1);

  typedef __attribute__((address_space(3))) const u32x2* lds_u2;
  typedef __attribute__((address_space(3))) const unsigned* lds_u;
  const lds_cptr kp = (lds_cptr)lds + r32 * 40 + 16 * hi;
  const lds_cptr kp1 = (lds_cptr)lds + r32 * 40 + 32;
  const lds_cptr vp = (lds_cptr)lds + QH + (4 * hi + ((lane & 15) >> 2)) * 40 + (((lane >> 4) & 1) ? 32 : (lane & 3) * 8);

#define LOADK(t, kf0, kf1)                                                                   \
  {                                                                                          \
    u32x2 a_ = *(lds_u2)(kp + (t) * 1280), b_ = *(lds_u2)(kp + (t) * 1280 + 8);              \
    kf0 = __builtin_bit_cast(bf16x8, (u32x4){a_[0], a_[1], b_[0], b_[1]});                   \
    kf1 = __builtin_bit_cast(bf16x8, (u32x4){*(lds_u)(kp1 + (t) * 1280), 0u, 0u, 0u});       \
  }
#define LOADV(t, vf0, vf1)                                                                   \
  {                                                                                          \
    vf0 = wfrag<320>(vp + (t) * 1280);                                                       \
    vf1 = wfrag<320>(vp + (t) * 1280 + 640);                                                 \
  }
#define EX(x) __builtin_amdgcn_exp2f(x)

  f32x16 negm;
  bf16x8 kf0, kf1, vf0, vf1;
  {
    LOADK(0, kf0, kf1);
    f32x16 s = MFMA(kf0, q0, (f32x16){});
    s = MFMA(kf1, q1, s);
    float mx = s[0];
#pragma unroll
    for (int r = 1; r < 16; ++r) mx = fmaxf(mx, s[r]);
    mx = fmaxf(mx, __shfl_xor(mx, 32));
#pragma unroll
    for (int r = 0; r < 16; ++r) negm[r] = -mx;
  }
  f32x16 o = {};
  f32x16 sA, sB;
  sA = MFMA(kf0, q0, negm);
  sA = MFMA(kf1, q1, sA);
  LOADK(1, kf0, kf1);
  LOADV(0, vf0, vf1);
#define LOADK2(P0, P1, OFF)                                                                  \
  {                                                                                          \
    u32x2 a_ = *(lds_u2)((P0) + (OFF));                                                      \
    asm volatile("" ::: "memory");                                                           \
    u32x2 b_ = *(lds_u2)((P0) + (OFF) + 8);                                                  \
    kf0 = __builtin_bit_cast(bf16x8, (u32x4){a_[0], a_[1], b_[0], b_[1]});                   \
    kf1 = __builtin_bit_cast(bf16x8, (u32x4){*(lds_u)((P1) + (OFF)), 0u, 0u, 0u});           \
  }
#define STEP(SC, SN, KO, VO, NX, PK, PV)                                                                         \
  {                                                                                                              \
    u32x4 p0, p1;                                                                                                \
    if (NX) SN = MFMA(kf0, q0, negm);                                                                            \
    SC[0] = EX(SC[0]); SC[1] = EX(SC[1]); SC[2] = EX(SC[2]); SC[3] = EX(SC[3]);                                  \
    SC[4] = EX(SC[4]); SC[5] = EX(SC[5]);                                                                        \
    p0[0] = pack2(SC[0], SC[1]); p0[1] = pack2(SC[2], SC[3]); PIN(p0); SBAR();                                   \
    if (NX) SN = MFMA(kf1, q1, SN);                                                                              \
    if (PK) LOADK2(kq, kq1, KO);                                                                                 \
    SC[6] = EX(SC[6]); SC[7] = EX(SC[7]); SC[8] = EX(SC[8]); SC[9] = EX(SC[9]);                                  \
    p0[2] = pack2(SC[4], SC[5]); p0[3] = pack2(SC[6], SC[7]); PIN(p0); SBAR();                                   \
    SC[10] = EX(SC[10]); SC[11] = EX(SC[11]);                                                                    \
    o = MFMA(vf0, __builtin_bit_cast(bf16x8, p0), o);                                                            \
    SC[12] = EX(SC[12]); SC[13] = EX(SC[13]);                                                                    \
    p1[0] = pack2(SC[8], SC[9]); p1[1] = pack2(SC[10], SC[11]); PIN(p1); SBAR();                                 \
    SC[14] = EX(SC[14]); SC[15] = EX(SC[15]);                                                                    \
    p1[2] = pack2(SC[12], SC[13]); PIN(p1); SBAR();                                                              \
    p1[3] = pack2(SC[14], SC[15]); PIN(p1); SBAR();                                                              \
    o = MFMA(vf1, __builtin_bit_cast(bf16x8, p1), o);                                                            \
    if (PV) { vf0 = wfrag<320>(vq + (VO)); vf1 = wfrag<320>(vq + (VO) + 640); }                                  \
    SBAR();                                                                                                      \
  }
  {
    lds_cptr kq = kp + 2560, kq1 = kp1 + 2560, vq = vp + 1280;
    for (int t = 0; t < 62; t += 2) {
      PIN(vq); PIN(kq); PIN(kq1);
      STEP(sA, sB, 0, 0, true, true, true);
      STEP(sB, sA, 1280, 1280, true, true, true);
      kq += 2560; kq1 += 2560; vq += 2560;
    }
    STEP(sA, sB, 0, 0, true, false, true);
    STEP(sB, sA, 0, 0, false, false, false);
  }
  STAMP(3, 2);
  float l = __shfl(o[10], r32);
  if (__any(!(l < 1.0e30f))) {
    float mrun = -INFINITY;
    o = (f32x16){};
    for (int t = 0; t < 64; ++t) {
      LOADK(t, kf0, kf1);
      LOADV(t, vf0, vf1);
      f32x16 s = MFMA(kf0, q0, (f32x16){});
      s = MFMA(kf1, q1, s);
      float mx = s[0];
#pragma unroll
      for (int r = 1; r < 16; ++r) mx = fmaxf(mx, s[r]);
      mx = fmaxf(mx, __shfl_xor(mx, 32));
      const float mnew = fmaxf(mrun, mx);
      const float alpha = __builtin_amdgcn_exp2f(mrun - mnew);
      mrun = mnew;
#pragma unroll
      for (int r = 0; r < 16; ++r) o[r] *= alpha;
      u32x4 p0, p1;
#pragma unroll
      for (int j = 0; j < 4; ++j) {
        p0[j] = pack2(__builtin_amdgcn_exp2f(s[2 * j] - mnew), __builtin_amdgcn_exp2f(s[2 * j + 1] - mnew));
        p1[j] = pack2(__builtin_amdgcn_exp2f(s[8 + 2 * j] - mnew), __builtin_amdgcn_exp2f(s[9 + 2 * j] - mnew));
      }
      o = MFMA(vf0, __builtin_bit_cast(bf16x8, p0), o);
      o = MFMA(vf1, __builtin_bit_cast(bf16x8, p1), o);
    }
    l = __shfl(o[10], r32);
  }
  const float rl = 1.0f / l;
  char* ob = ws + WS_O + ((size_t)(q >> 5) * 36 * 64 + (q & 31)) * 16;
#pragma unroll
  for (int i = 0; i < 5; ++i) {
    const int d = (i >> 1) * 8 + 4 * hi + (i & 1) * 2;
    const int r = (i >> 1) * 4 + (i & 1) * 2;
    const int f = head * DH_ + d;
    if (i < 4 || !hi) *(unsigned*)(ob + ((f >> 4) * 64 + ((f >> 3) & 1) * 32) * 16 + (f & 7) * 2) = pack2(o[r] * rl, o[r + 1] * rl);
  }
  STAMP(3, 20);
}

extern "C" void kernel_launch(void* const* d_in, const int* in_sizes, int n_in, void* d_out, int out_size,
                              void* d_ws, size_t ws_size, hipStream_t stream) {
  const u16* x = (const u16*)d_in[0];
  const u16* wqkv = (const u16*)d_in[1];
  const u16* wo = (const u16*)d_in[2];
  const u16* w1 = (const u16*)d_in[4];
  const u16* w3 = (const u16*)d_in[5];
  const u16* w2 = (const u16*)d_in[6];
  char* ws = (char*)d_ws;
  u16* out = (u16*)d_out;
  (void)hipFuncSetAttribute((const void*)k_gemm1, hipFuncAttributeMaxDynamicSharedMemorySize, GEMM_LDS);
  (void)hipFuncSetAttribute((const void*)k_down, hipFuncAttributeMaxDynamicSharedMemorySize, GEMM_LDS);
  (void)hipFuncSetAttribute((const void*)k_final, hipFuncAttributeMaxDynamicSharedMemorySize, GEMM_LDS);
  (void)hipFuncSetAttribute((const void*)k_attn, hipFuncAttributeMaxDynamicSharedMemorySize, ATTN_LDS);
  k_ln<<<256, 64, 0, stream>>>(x, ws);
  k_gemm1<<<256, 512, GEMM_LDS, stream>>>(wqkv, w1, w3, ws, (const char*)w2, (const char*)wo);
  k_down<<<256, 512, GEMM_LDS, stream>>>(w2, ws);
  k_attn<<<256, 768, ATTN_LDS, stream>>>(ws);
  k_final<<<288, 128, 2 * SLOT_B + 16, stream>>>(wo, ws, x, out);
}
```

```cpp
#include <hip/hip_runtime.h>
#include <hip/hip_bf16.h>
#include <stdint.h>

typedef unsigned short u16;
typedef short bf16x8 __attribute__((ext_vector_type(8)));
typedef short s16x4 __attribute__((ext_vector_type(4)));
typedef float f32x16 __attribute__((ext_vector_type(16)));
typedef float f32x4 __attribute__((ext_vector_type(4)));
typedef float f32x2 __attribute__((ext_vector_type(2)));
typedef unsigned u32x4 __attribute__((ext_vector_type(4)));
typedef unsigned u32x2 __attribute__((ext_vector_type(2)));
typedef __bf16 bf16x2_t __attribute__((ext_vector_type(2)));
typedef short v4i16_t __attribute__((ext_vector_type(4)));
typedef __attribute__((address_space(3))) const char* lds_cptr;
typedef __attribute__((address_space(3))) char* lds_ptr;

#define DI __device__ __forceinline__
#define MFMA(a, b, c) __builtin_amdgcn_mfma_f32_32x32x16_bf16((a), (b), (c), 0, 0, 0)
#define SBAR() __builtin_amdgcn_sched_barrier(0)

constexpr int S_ = 2048, D_ = 576, H_ = 32, DH_ = 18, F_ = 4608;
constexpr size_t WS_XN = 0;
constexpr size_t WS_Q = WS_XN + (size_t)S_ * D_ * 2;
constexpr size_t QH = 81920;
constexpr size_t WS_KV = WS_Q + (size_t)H_ * QH;
constexpr size_t WS_H = WS_KV + (size_t)H_ * 2 * QH;
constexpr size_t WS_O = WS_H + (size_t)S_ * F_ * 2;
constexpr size_t WS_P = WS_O + (size_t)S_ * D_ * 2;
constexpr size_t WS_PS = WS_P + (size_t)8 * S_ * D_ * 4;
constexpr size_t WS_END = WS_PS + (size_t)S_ * D_ * 4;

#define STAMP(kid, i) do {} while (0)
DI unsigned pack2(float a, float b) {
  bf16x2_t r = __builtin_convertvector((f32x2){a, b}, bf16x2_t);
  return __builtin_bit_cast(unsigned, r);
}
DI void st16_wt(char* ws, size_t off, u32x4 v) {
  __builtin_amdgcn_raw_buffer_store_b128(v, __builtin_amdgcn_make_buffer_rsrc(ws, 0, 0x7fffffff, 0x00020000), (int)off, 0, 17);
}
DI float bflo(unsigned w) { return __uint_as_float(w << 16); }
DI float bfhi(unsigned w) { return __uint_as_float(w & 0xffff0000u); }

DI void glds16(const void* g, unsigned lds_base) {
  unsigned sv;
  asm volatile("s_mov_b32 %0, m0\n\ts_mov_b32 m0, %2\n\ts_nop 0\n\tglobal_load_lds_dwordx4 %1, off\n\ts_mov_b32 m0, %0"
               : "=&s"(sv) : "v"(g), "s"(lds_base) : "memory");
}
DI s16x4 vtr(lds_cptr p) {
  return __builtin_bit_cast(s16x4, __builtin_amdgcn_ds_read_tr16_b64_v4i16((__attribute__((address_space(3))) v4i16_t*)p));
}
template <int OFF2>
DI bf16x8 wfrag(lds_cptr p) {
  s16x4 lo = vtr(p), hi = vtr(p + OFF2);
  return (bf16x8){lo[0], lo[1], lo[2], lo[3], hi[0], hi[1], hi[2], hi[3]};
}
#define WAITV_BAR(N) asm volatile("s_waitcnt vmcnt(" #N ")\n\ts_barrier" ::: "memory")
#define WAITV(N) asm volatile("s_waitcnt vmcnt(" #N ")" ::: "memory")
#define WAITVN(N) asm volatile("s_waitcnt vmcnt(%0)" :: "i"(N) : "memory")
#define WAIT_BAR(N) asm volatile("s_waitcnt vmcnt(" #N ") lgkmcnt(0)\n\ts_barrier" ::: "memory")

__global__ __launch_bounds__(64) void k_ln(const u16* __restrict__ x, char* __restrict__ ws) {
  const int lane = threadIdx.x & 63, wid = threadIdx.x >> 6;
  const int row = (blockIdx.x + wid) * 8 + (lane >> 3), c8 = lane & 7;
  const u16* xr = x + (size_t)row * D_ + c8 * 8;
  u32x4 a[9];
#pragma unroll
  for (int j = 0; j < 9; ++j) a[j] = *(const u32x4*)(xr + 64 * j);
  float s = 0.f;
#pragma unroll
  for (int j = 0; j < 9; ++j)
#pragma unroll
    for (int i = 0; i < 4; ++i) s += bflo(a[j][i]) + bfhi(a[j][i]);
  s += __shfl_xor(s, 1); s += __shfl_xor(s, 2); s += __shfl_xor(s, 4);
  const float mean = s * (1.0f / D_);
  float q = 0.f;
#pragma unroll
  for (int j = 0; j < 9; ++j)
#pragma unroll
    for (int i = 0; i < 4; ++i) { float d0 = bflo(a[j][i]) - mean, d1 = bfhi(a[j][i]) - mean; q += d0 * d0 + d1 * d1; }
  q += __shfl_xor(q, 1); q += __shfl_xor(q, 2); q += __shfl_xor(q, 4);
  const float mb = bflo(pack2(mean, mean));
  const float vb = bflo(pack2(q * (1.0f / D_), 0.f));
  const float sd = bflo(pack2(sqrtf(bflo(pack2(vb + 1e-5f, 0.f))), 0.f));
  char* xn = ws + WS_XN + ((size_t)(row >> 5) * 36 * 64 + (row & 31)) * 16;
#pragma unroll
  for (int j = 0; j < 9; ++j) {
    u32x4 o;
#pragma unroll
    for (int i = 0; i < 4; ++i) {
      const unsigned dd = pack2(bflo(a[j][i]) - mb, bfhi(a[j][i]) - mb);
      o[i] = pack2(bflo(dd) / sd, bfhi(dd) / sd);
    }
    const int c = c8 + 8 * j;
    *(u32x4*)(xn + ((c >> 1) * 64 + (c & 1) * 32) * 16) = o;
  }
}

constexpr int NSLOT = 4, SLOT_B = 36864;
constexpr int GEMM_LDS = NSLOT * SLOT_B + 16 + 8192;

template <int V> struct IntC { static constexpr int value = V; };
#define PIN(x) asm volatile("" : "+v"(x))

template <int MODE, int NW>
DI void gemm_core(const char* __restrict__ Af  ,
                  int ngu, int gu0, int ndt, int dt0,
                  const u16* __restrict__ W0, const u16* __restrict__ W1, const u16* __restrict__ W2,
                  char* __restrict__ ws, const u16* __restrict__ xin, u16* __restrict__ out,
                  int rowblk  , int kslice, char* lds, int cnt_off, int tail) {
  const int tid = threadIdx.x, lane = tid & 63, r32 = lane & 31, hi = lane >> 5;
  const int wid = __builtin_amdgcn_readfirstlane(tid >> 6);
  const unsigned lds0 = (unsigned)(uintptr_t)lds;
  const int nchunk = 4 * ngu + 2 * ndt + (tail >= 0 ? 2 : 0);
  STAMP(MODE, 0);
  typedef __attribute__((address_space(3))) volatile unsigned* lds_vu;
  const lds_vu cnt = (lds_vu)((lds_ptr)lds + cnt_off);
  if (tid == 0) *cnt = 0;

  auto chunk_src = [&](int c, int& ld) __attribute__((always_inline)) -> const u16* {
    const u16* base;
    if (MODE == 0) {
      if (c < 4 * ngu) { base = ((c & 2) ? W2 : W1) + 64 * (gu0 + (c >> 2)); ld = F_; }
      else { base = W0 + 64 * (dt0 + ((c - 4 * ngu) >> 1)); ld = 3 * D_; }
    } else if (MODE == 1) {
      base = W0 + (size_t)kslice * 576 * D_ + 64 * ((c >> 1) < ndt ? dt0 + (c >> 1) : (tail >> 1)); ld = D_;
    } else {
      base = W0 + 64 * (dt0 + (c >> 1)); ld = D_;
    }
    const int row = wid * 8 + (lane >> 3);
    return base + (size_t)((c & 1) * 288 + row) * ld + ((lane & 7) ^ (((lane >> 4) & 1) << 2)) * 8;
  };
  constexpr int NPIECE = (36 + NW - 1) / NW;
  auto issue = [&](int c) __attribute__((always_inline)) {
    int ld; const u16* src = chunk_src(c, ld);
    const unsigned dst = lds0 + (c % NSLOT) * SLOT_B + wid * 1024;
#pragma unroll
    for (int i = 0; i < NPIECE; ++i)
      if (i < 36 / NW || wid < 36 % NW)
        glds16(src + (size_t)i * NW * 8 * ld, (unsigned)__builtin_amdgcn_readfirstlane(dst + i * NW * 1024));
  };
  u32x2 pfx[8]; f32x4 pfm[8];
  if (MODE == 2) {
#pragma unroll
    for (int i = 0; i < 8; ++i) {
      const int t = 2 * dt0 + (i >> 2), g = i & 3;
      pfx[i] = *(const u32x2*)(xin + (size_t)(rowblk * 32 + r32) * D_ + 32 * t + 8 * g + 4 * hi);
      pfm[i] = *(const f32x4*)(ws + WS_PS + ((((size_t)rowblk * 18 + t) * 4 + g) * 64 + lane) * 16);
    }
  }
  issue(0);
  issue(1);
  bf16x8 A[36];
  const char* ap = Af + lane * 16;
#define ALOAD(k) asm volatile("global_load_dwordx4 %0, %1, off offset:%2" : "=v"(A[k]) : "v"(ap + ((k) >> 2) * 4096), "i"(((k) & 3) * 1024) : "memory")
#pragma unroll
  for (int s = 0; s < 6; ++s) ALOAD(s);
  asm volatile("s_waitcnt vmcnt(%0) lgkmcnt(0)\n\ts_barrier" :: "i"(6 + (NW == 8 ? 4 : 18)) : "memory");
  SBAR();
  STAMP(MODE, 1);

  constexpr int OFF2 = (MODE == 1) ? 1024 : 512;
  const int q4 = (lane & 15) >> 2;
  const int rdo = ((MODE == 1 ? hi * 4 : hi * 8) + q4) * 128 + ((q4 >> 1) << 6) + ((lane >> 4) & 1) * 32 + (lane & 3) * 8;
  const lds_cptr rdA = (lds_cptr)lds + rdo, rdB = (lds_cptr)lds + (rdo ^ 64);
  const int dB = (rdo ^ 64) - rdo;
  const int m = rowblk * 32 + r32;

  auto silu = [&](float g) __attribute__((always_inline)) -> float { return g * __builtin_amdgcn_rcpf(1.0f + __builtin_amdgcn_exp2f(-1.4426950408889634f * g)); };
  auto epi_h = [&](const u32x4& sa, const u32x4& sb, const f32x16& up, int t32) __attribute__((always_inline)) {
    const size_t off = WS_H + (((size_t)rowblk * 288 + 2 * t32) * 64 + lane) * 16;
    u32x4 w0, w1;
#pragma unroll
    for (int j = 0; j < 4; ++j) {
      w0[j] = pack2(bflo(sa[j]) * up[2 * j], bfhi(sa[j]) * up[2 * j + 1]);
      w1[j] = pack2(bflo(sb[j]) * up[8 + 2 * j], bfhi(sb[j]) * up[9 + 2 * j]);
    }
    st16_wt(ws, off, w0);
    st16_wt(ws, off + 1024, w1);
  };
  auto epi_single = [&](const f32x16& a, int t) __attribute__((always_inline)) {
    if (MODE == 0) {
      const lds_ptr stg = (lds_ptr)lds + NSLOT * SLOT_B + 16 + wid * 1024;
      const int n = 32 * t + 2 * (lane & 15);
      const int which = n / D_, f = n - which * D_, head = f / DH_, d = f - head * DH_;
      const size_t off = (which == 0 ? (WS_Q + (size_t)head * QH) : (WS_KV + (size_t)head * 2 * QH + (size_t)(which - 1) * QH)) +
                         (size_t)(rowblk * 32 + (lane >> 4)) * 40 + d * 2;
      const float sc = (32 * t < D_) ? (0.23570226039551584f * 1.4426950408889634f) : 1.0f;
#pragma unroll
      for (int rho = 0; rho < 2; ++rho) {
        if ((r32 >> 4) == rho) {
#pragma unroll
          for (int g = 0; g < 4; ++g)
            *(__attribute__((address_space(3))) u32x2*)(stg + (r32 & 15) * 64 + (8 * g + 4 * hi) * 2) =
                (u32x2){pack2(a[4 * g] * sc, a[4 * g + 1] * sc), pack2(a[4 * g + 2] * sc, a[4 * g + 3] * sc)};
        }
        asm volatile("s_waitcnt lgkmcnt(0)" ::: "memory");
        unsigned wv[4];
#pragma unroll
        for (int i = 0; i < 4; ++i) wv[i] = *(__attribute__((address_space(3))) const unsigned*)(stg + ((lane >> 4) + 4 * i) * 64 + (lane & 15) * 4);
#pragma unroll
        for (int i = 0; i < 4; ++i) *(unsigned*)(ws + off + (size_t)(16 * rho + 4 * i) * 40) = wv[i];
        asm volatile("s_waitcnt lgkmcnt(0)" ::: "memory");
      }
    } else if (MODE == 1) {
      const size_t off = WS_P + (((((size_t)kslice * 64 + rowblk) * 18 + t) * 4) * 64 + lane) * 16;
#pragma unroll
      for (int g = 0; g < 4; ++g)
        st16_wt(ws, off + g * 1024, __builtin_bit_cast(u32x4, (f32x4){a[4 * g], a[4 * g + 1], a[4 * g + 2], a[4 * g + 3]}));
    } else {
#pragma unroll
      for (int g = 0; g < 4; ++g) {
        const int col = 32 * t + 8 * g + 4 * hi;
        const u32x2 xv = pfx[(t & 1) * 4 + g];
        const f32x4 pm = pfm[(t & 1) * 4 + g];
        f32x4 sum = {a[4 * g] + pm[0] + bflo(xv[0]), a[4 * g + 1] + pm[1] + bfhi(xv[0]),
                     a[4 * g + 2] + pm[2] + bflo(xv[1]), a[4 * g + 3] + pm[3] + bfhi(xv[1])};
        *(u32x2*)(out + (size_t)m * D_ + col) = (u32x2){pack2(sum[0], sum[1]), pack2(sum[2], sum[3])};
      }
    }
  };

  bf16x8 bq[6];
#pragma unroll
  for (int s = 0; s < 5; ++s) bq[s] = wfrag<OFF2>(rdA + s * 2048);

  int c = 0;
  auto run_chunk = [&](f32x16& aX, f32x16& aY, auto half_, auto sil_, auto nst_, auto start_, f32x16& sg, u32x4& s0, u32x4& s1, int nrt) __attribute__((always_inline)) {
    constexpr int HALF = decltype(half_)::value, SIL = decltype(sil_)::value, NST = decltype(nst_)::value, START = decltype(start_)::value;
    int so = __builtin_amdgcn_readfirstlane((c % NSLOT) * SLOT_B);
    const int cn = c + 1 < nchunk ? c + 1 : c;
    int sn = __builtin_amdgcn_readfirstlane((cn % NSLOT) * SLOT_B);
    asm volatile("" : "+s"(so), "+s"(sn));
    const lds_cptr pn = rdA + sn + ((MODE == 1 && tail >= 0 && c + 3 == nchunk && (tail & 1)) ? dB : 0);
    const bool doiss = START != 1 && c + 3 < nchunk;
    int nld; const u16* nsrc = chunk_src(doiss ? c + 3 : c, nld);
    const size_t nstr = (size_t)NW * 8 * nld;
    const unsigned ndst = (unsigned)__builtin_amdgcn_readfirstlane(lds0 + ((c + 3) % NSLOT) * SLOT_B + wid * 1024);
    const unsigned target = (unsigned)(NW * (c + 1));
    unsigned fl = 0;
    float sv[2], e1[2], e2[2];
#pragma unroll
    for (int s = 0; s < 36; ++s) {
      if (NW == 8 && s == 0) __builtin_amdgcn_s_setprio(2);
      if (NW == 8 && s == 18) __builtin_amdgcn_s_setprio(0);
      if (NW == 8 && s == (START == 2 ? 20 : 1)) {
        PIN(aX); PIN(aY);
        if (START == 2) {
          if (c + 2 < nchunk) { WAITV(4); } else { WAITV(0); }
        } else if (START == 0) {
          if (c + 2 < nchunk) {
            if (NST >= 0) { if (wid < 4) { WAITVN(5 + (NST >= 0 ? NST : 0)); } else { WAITVN(4 + (NST >= 0 ? NST : 0)); } }
            else if (nrt == 16) { if (wid < 4) { WAITVN(21); } else { WAITVN(20); } }
            else { if (wid < 4) { WAITVN(9); } else { WAITVN(8); } }
          } else { WAITV(0); }
        }
        if (lane == 0) __hip_atomic_fetch_add((__attribute__((address_space(3))) unsigned*)((lds_ptr)lds + cnt_off), 1u, __ATOMIC_RELAXED, __HIP_MEMORY_SCOPE_WORKGROUP);
      }
      if (NW != 8 && START == 1 && s == 26) { PIN(aX); PIN(aY); asm volatile("s_barrier" ::: "memory"); }
      if (NW == 8 && s == 23) fl = *cnt;
      if (NW == 8 && s == 26) {
        PIN(aX); PIN(aY);
        while ((unsigned)__builtin_amdgcn_readfirstlane(fl) < target) { __builtin_amdgcn_s_sleep(1); fl = *cnt; }
        asm volatile("" ::: "memory");
      }
      if (NW == 8 && s >= 27 && (s - 27) % 2 == 0) {
        const int i = (s - 27) / 2;
        PIN(aY);
        if (doiss && (i < 4 || wid < 4)) glds16(nsrc + i * nstr, ndst + i * NW * 1024);
      }
      {
        const int sp = s + 5;
        if (sp < 18) bq[sp % 6] = wfrag<OFF2>(rdA + so + sp * 2048);
        else if (sp < 36) bq[sp % 6] = wfrag<OFF2>(rdB + so + (sp - 18) * 2048);
        else bq[sp % 6] = wfrag<OFF2>(pn + (sp - 36) * 2048);
      }
      if (START == 1 && s < 18) asm volatile("s_waitcnt vmcnt(%1)" : "+v"(A[s]) : "i"(s <= 15 ? 5 + s : 35 - s));
      if (START == 2 && s < 18) asm volatile("s_waitcnt vmcnt(%1)" : "+v"(A[18 + s]) : "i"(17 - s + (NW == 8 ? 8 : 0)));
      if (s < 18) {
        if (HALF == 0 && s == 0) aX = MFMA(bq[0], A[0], (f32x16){});
        else aX = MFMA(bq[s % 6], A[18 * HALF + s], aX);
      } else {
        if (HALF == 0 && s == 18) aY = MFMA(bq[s % 6], A[0], (f32x16){});
        else aY = MFMA(bq[s % 6], A[18 * HALF + s - 18], aY);
      }
      if (START == 1) {
        if (s <= 14) { ALOAD(6 + 2 * s); ALOAD(7 + 2 * s); }
        if (NW == 8 && s >= 18 && s < 28) {
          const int cc = 2 + (s - 18) / 5, i = (s - 18) % 5;
          int ld2; const u16* src2 = chunk_src(cc, ld2);
          if (i < 4 || wid < 4) glds16(src2 + (size_t)i * NW * 8 * ld2, (unsigned)__builtin_amdgcn_readfirstlane(lds0 + cc * SLOT_B + wid * 1024 + i * NW * 1024));
        }
      }
      if (SIL != 0) {
        constexpr int B0 = (SIL == 1) ? 18 : 0;
        const f32x16& g = (SIL == 1) ? aX : sg;
        const int r1 = s - B0, r2 = s - B0 - 1, r3 = s - B0 - 2;
        if (r3 >= 0 && r3 < 16) {
          sv[r3 & 1] = g[r3] * e2[r3 & 1];
          if (r3 & 1) { unsigned w = pack2(sv[0], sv[1]); PIN(w); if (r3 < 8) s0[r3 >> 1] = w; else s1[(r3 - 8) >> 1] = w; }
        }
        if (r2 >= 0 && r2 < 16) { e2[r2 & 1] = __builtin_amdgcn_rcpf(1.0f + e1[r2 & 1]); PIN(e2[r2 & 1]); }
        if (r1 >= 0 && r1 < 16) { e1[r1 & 1] = __builtin_amdgcn_exp2f(-1.4426950408889634f * g[r1]); PIN(e1[r1 & 1]); }
      }
      SBAR();
    }
    ++c;
    STAMP(MODE, 1 + c);
  };

  auto run_tail = [&](f32x16& acc) __attribute__((always_inline)) {
    int so = __builtin_amdgcn_readfirstlane((c % NSLOT) * SLOT_B), sn = __builtin_amdgcn_readfirstlane(((c + 1) % NSLOT) * SLOT_B);
    asm volatile("" : "+s"(so), "+s"(sn));
    const lds_cptr rdO = rdA + ((tail & 1) ? dB : 0);
    const unsigned target = (unsigned)(NW * (c + 1));
    unsigned fl = 0;
#pragma unroll
    for (int s = 0; s < 36; ++s) {
      if (s == 1) {
        PIN(acc);
        WAITV(0);
        if (lane == 0) __hip_atomic_fetch_add((__attribute__((address_space(3))) unsigned*)((lds_ptr)lds + cnt_off), 1u, __ATOMIC_RELAXED, __HIP_MEMORY_SCOPE_WORKGROUP);
      }
      if (s == 9) fl = *cnt;
      if (s == 12) {
        PIN(acc);
        while ((unsigned)__builtin_amdgcn_readfirstlane(fl) < target) { __builtin_amdgcn_s_sleep(1); fl = *cnt; }
        asm volatile("" ::: "memory");
      }
      const int sp = s + 5;
      if (sp < 18) bq[sp % 6] = wfrag<OFF2>(rdO + so + sp * 2048);
      else if (sp < 36) bq[sp % 6] = wfrag<OFF2>(rdO + sn + (sp - 18) * 2048);
      if (s == 0) acc = MFMA(bq[0], A[0], (f32x16){});
      else acc = MFMA(bq[s % 6], A[s], acc);
      SBAR();
    }
  };

  f32x16 a0, a1;
  u32x4 sa0, sa1, sb0, sb1;
  typedef IntC<0> I0; typedef IntC<1> I1; typedef IntC<2> I2;
  if (MODE == 0) {
    run_chunk(a0, a1, I0{}, I0{}, I0{}, I1{}, a1, sa0, sa1, 0);
    run_chunk(a0, a1, I1{}, I1{}, I0{}, I2{}, a1, sa0, sa1, 0);
    for (int gi = 0; gi < ngu; ++gi) {
      run_chunk(a0, a1, I0{}, I2{}, I0{}, I0{}, a1, sb0, sb1, 0);
      run_chunk(a0, a1, I1{}, I0{}, I0{}, I0{}, a1, sb0, sb1, 0);
      epi_h(sa0, sa1, a0, 2 * (gu0 + gi));
      epi_h(sb0, sb1, a1, 2 * (gu0 + gi) + 1);
      if (gi + 1 < ngu) {
        run_chunk(a0, a1, I0{}, I0{}, IntC<4>{}, I0{}, a1, sa0, sa1, 0);
        run_chunk(a0, a1, I1{}, I1{}, I0{}, I0{}, a1, sa0, sa1, 0);
      }
    }
    for (int di = 0; di < ndt; ++di) {
      run_chunk(a0, a1, I0{}, I0{}, IntC<-1>{}, I0{}, a1, sa0, sa1, di ? 16 : 4);
      run_chunk(a0, a1, I1{}, I0{}, I0{}, I0{}, a1, sa0, sa1, 0);
      epi_single(a0, 2 * (dt0 + di));
      epi_single(a1, 2 * (dt0 + di) + 1);
    }
  } else {
    run_chunk(a0, a1, I0{}, I0{}, I0{}, I1{}, a1, sa0, sa1, 0);
    run_chunk(a0, a1, I1{}, I0{}, I0{}, I2{}, a1, sa0, sa1, 0);
    epi_single(a0, 2 * dt0);
    epi_single(a1, 2 * dt0 + 1);
    for (int di = 1; di < ndt; ++di) {
      run_chunk(a0, a1, I0{}, I0{}, IntC<8>{}, I0{}, a1, sa0, sa1, 0);
      run_chunk(a0, a1, I1{}, I0{}, I0{}, I0{}, a1, sa0, sa1, 0);
      epi_single(a0, 2 * (dt0 + di));
      epi_single(a1, 2 * (dt0 + di) + 1);
    }
    if (MODE == 1 && tail >= 0) {
      run_tail(a0);
      epi_single(a0, tail);
    }
  }
  STAMP(MODE, 20);
}

__global__ __launch_bounds__(512, 2) void k_gemm1(const u16* __restrict__ wqkv, const u16* __restrict__ w1,
                                                  const u16* __restrict__ w3, char* __restrict__ ws,
                                                  const char* __restrict__ w2b, const char* __restrict__ wob) {
  extern __shared__ __attribute__((aligned(16))) char lds[];
  const int b = blockIdx.x, xcd = b & 7, idx = b >> 3;
  const int panel = idx & 7, cg = xcd * 4 + (idx >> 3);
  int ngu, gu0, ndt, dt0;
  if (cg < 11) { ngu = 3; gu0 = 3 * cg; ndt = 0; dt0 = 0; }
  else if (cg < 29) { const int j = cg - 11; ngu = 2; gu0 = 33 + 2 * j; ndt = 1; dt0 = j; }
  else { const int j = cg - 29; ngu = 1; gu0 = 69 + j; ndt = 3; dt0 = 18 + 3 * j; }
  const int rowblk = panel * 8 + (threadIdx.x >> 6);
  gemm_core<0, 8>(ws + WS_XN + (size_t)rowblk * 36 * 1024, ngu, gu0, ndt, dt0, wqkv, w1, w3, ws, nullptr, nullptr,
                  rowblk, 0, lds, NSLOT * SLOT_B, -1);
  if (cg >= 11 && cg < 29) {
    const int w = ((cg - 11) * 8 + panel) * 8 + (threadIdx.x >> 6);
    const int lane = threadIdx.x & 63;
    u32x4 acc = {0, 0, 0, 0};
#pragma unroll
    for (int i = 0; i < 6; ++i) {
      const int pc = w + 1152 * i;
      if (pc < 5184) acc ^= *(const u32x4*)(w2b + (size_t)pc * 1024 + lane * 16);
      else if (pc < 5832) acc ^= *(const u32x4*)(wob + (size_t)(pc - 5184) * 1024 + lane * 16);
    }
    asm volatile("" :: "v"(acc));
  }
}

__global__ __launch_bounds__(512, 2) void k_down(const u16* __restrict__ w2, char* __restrict__ ws) {
  extern __shared__ __attribute__((aligned(16))) char lds[];
  const int b = blockIdx.x, ks = b & 7, idx = b >> 3;
  const int panel = idx & 7, nq = idx >> 3;
  const int ndt = 2, dt0 = 2 * nq, tail = nq < 2 ? 16 + nq : -1;
  const int rowblk = panel * 8 + (threadIdx.x >> 6);
  gemm_core<1, 8>(ws + WS_H + ((size_t)rowblk * 288 + ks * 36) * 1024, 0, 0, ndt, dt0, w2, nullptr, nullptr, ws, nullptr,
                  nullptr, rowblk, ks, lds, NSLOT * SLOT_B, tail);
}

__global__ __launch_bounds__(128, 2) void k_final(const u16* __restrict__ wo, char* __restrict__ ws,
                                                  const u16* __restrict__ x, u16* __restrict__ out) {
  extern __shared__ __attribute__((aligned(16))) char lds[];
  const int b = blockIdx.x, rp = b & 31, u = b >> 5;
  const int rowblk = rp * 2 + (threadIdx.x >> 6);
  gemm_core<2, 2>(ws + WS_O + (size_t)rowblk * 36 * 1024, 0, 0, 1, u, wo, nullptr, nullptr, ws, x, out, rowblk, 0, lds, 2 * SLOT_B, -1);
}

constexpr int ATTN_LDS = 163840;

__global__ __launch_bounds__(768, 3) void k_attn(char* __restrict__ ws) {
  extern __shared__ __attribute__((aligned(16))) char lds[];
  const int tid = threadIdx.x, lane = tid & 63, r32 = lane & 31, hi = lane >> 5;
  const int wid = __builtin_amdgcn_readfirstlane(tid >> 6);
  const int b = blockIdx.x, xcd = b & 7, idx = b >> 3;
  const int head = xcd * 4 + (idx >> 3), qb = idx & 7;
  const unsigned lds0 = (unsigned)(uintptr_t)lds;
  STAMP(3, 0);
  if (wid >= 8) {
    WAITV_BAR(0);
    WAIT_BAR(0);
    for (int pc = wid - 8; pc < 18; pc += 4) {
      const size_t off = ((size_t)b * 18 + pc) * 1024 + lane * 16;
      f32x4 v[8];
#pragma unroll
      for (int sl = 0; sl < 8; ++sl) v[sl] = __builtin_nontemporal_load((const f32x4*)(ws + WS_P + (size_t)sl * (64 * 18 * 4 * 1024) + off));
      f32x4 sum = ((v[0] + v[1]) + (v[2] + v[3])) + ((v[4] + v[5]) + (v[6] + v[7]));
      *(f32x4*)(ws + WS_PS + off) = sum;
    }
    return;
  }
  {
    const char* src = ws + WS_KV + (size_t)head * 2 * QH + wid * 1024 + lane * 16;
#pragma unroll
    for (int i = 0; i < 20; ++i) glds16(src + i * 8192, (unsigned)__builtin_amdgcn_readfirstlane(lds0 + wid * 1024 + i * 8192));
  }
  const int q = qb * 256 + wid * 32 + r32;
  const char* qp = ws + WS_Q + (size_t)head * QH + (size_t)q * 40;
  u32x2 qa = *(const u32x2*)(qp + 16 * hi), qb2 = *(const u32x2*)(qp + 16 * hi + 8);
  unsigned q1w = *(const unsigned*)(qp + 32);
  asm volatile("" : "+v"(qa), "+v"(qb2), "+v"(q1w));
  WAITV_BAR(0);
  if (hi) q1w = 0;
  const bf16x8 q0 = __builtin_bit_cast(bf16x8, (u32x4){qa[0], qa[1], qb2[0], qb2[1]});
  const bf16x8 q1 = __builtin_bit_cast(bf16x8, (u32x4){q1w, 0u, 0u, 0u});
#pragma unroll
  for (int i = 0; i < 4; ++i) *(__attribute__((address_space(3))) unsigned*)((lds_ptr)lds + QH + (size_t)(tid + 512 * i) * 40 + 36) = 0x00003f80u;
  WAIT_BAR(0);
  STAMP(3, 1);

  typedef __attribute__((address_space(3))) const u32x2* lds_u2;
  typedef __attribute__((address_space(3))) const unsigned* lds_u;
  const lds_cptr kp = (lds_cptr)lds + r32 * 40 + 16 * hi;
  const lds_cptr kp1 = (lds_cptr)lds + r32 * 40 + 32;
  const lds_cptr vp = (lds_cptr)lds + QH + (4 * hi + ((lane & 15) >> 2)) * 40 + (((lane >> 4) & 1) ? 32 : (lane & 3) * 8);

#define LOADK(t, kf0, kf1)                                                                   \
  {                                                                                          \
    u32x2 a_ = *(lds_u2)(kp + (t) * 1280), b_ = *(lds_u2)(kp + (t) * 1280 + 8);              \
    kf0 = __builtin_bit_cast(bf16x8, (u32x4){a_[0], a_[1], b_[0], b_[1]});                   \
    kf1 = __builtin_bit_cast(bf16x8, (u32x4){*(lds_u)(kp1 + (t) * 1280), 0u, 0u, 0u});       \
  }
#define LOADV(t, vf0, vf1)                                                                   \
  {                                                                                          \
    vf0 = wfrag<320>(vp + (t) * 1280);                                                       \
    vf1 = wfrag<320>(vp + (t) * 1280 + 640);                                                 \
  }
#define EX(x) __builtin_amdgcn_exp2f(x)

  f32x16 negm;
  bf16x8 kf0, kf1, vf0, vf1;
  {
    LOADK(0, kf0, kf1);
    f32x16 s = MFMA(kf0, q0, (f32x16){});
    s = MFMA(kf1, q1, s);
    float mx = s[0];
#pragma unroll
    for (int r = 1; r < 16; ++r) mx = fmaxf(mx, s[r]);
    mx = fmaxf(mx, __shfl_xor(mx, 32));
#pragma unroll
    for (int r = 0; r < 16; ++r) negm[r] = -mx;
  }
  f32x16 o = {};
  f32x16 sA, sB;
  sA = MFMA(kf0, q0, negm);
  sA = MFMA(kf1, q1, sA);
  LOADK(1, kf0, kf1);
  LOADV(0, vf0, vf1);
#define LOADK2(P0, P1, OFF)                                                                  \
  {                                                                                          \
    u32x2 a_ = *(lds_u2)((P0) + (OFF));                                                      \
    asm volatile("" ::: "memory");                                                           \
    u32x2 b_ = *(lds_u2)((P0) + (OFF) + 8);                                                  \
    kf0 = __builtin_bit_cast(bf16x8, (u32x4){a_[0], a_[1], b_[0], b_[1]});                   \
    kf1 = __builtin_bit_cast(bf16x8, (u32x4){*(lds_u)((P1) + (OFF)), 0u, 0u, 0u});           \
  }
#define STEP(SC, SN, KO, VO, NX, PK, PV)                                                                         \
  {                                                                                                              \
    u32x4 p0, p1;                                                                                                \
    if (NX) SN = MFMA(kf0, q0, negm);                                                                            \
    SC[0] = EX(SC[0]); SC[1] = EX(SC[1]); SC[2] = EX(SC[2]); SC[3] = EX(SC[3]);                                  \
    SC[4] = EX(SC[4]); SC[5] = EX(SC[5]);                                                                        \
    p0[0] = pack2(SC[0], SC[1]); p0[1] = pack2(SC[2], SC[3]); PIN(p0); SBAR();                                   \
    if (NX) SN = MFMA(kf1, q1, SN);                                                                              \
    if (PK) LOADK2(kq, kq1, KO);                                                                                 \
    SC[6] = EX(SC[6]); SC[7] = EX(SC[7]); SC[8] = EX(SC[8]); SC[9] = EX(SC[9]);                                  \
    p0[2] = pack2(SC[4], SC[5]); p0[3] = pack2(SC[6], SC[7]); PIN(p0); SBAR();                                   \
    SC[10] = EX(SC[10]); SC[11] = EX(SC[11]);                                                                    \
    o = MFMA(vf0, __builtin_bit_cast(bf16x8, p0), o);                                                            \
    SC[12] = EX(SC[12]); SC[13] = EX(SC[13]);                                                                    \
    p1[0] = pack2(SC[8], SC[9]); p1[1] = pack2(SC[10], SC[11]); PIN(p1); SBAR();                                 \
    SC[14] = EX(SC[14]); SC[15] = EX(SC[15]);                                                                    \
    p1[2] = pack2(SC[12], SC[13]); PIN(p1); SBAR();                                                              \
    p1[3] = pack2(SC[14], SC[15]); PIN(p1); SBAR();                                                              \
    o = MFMA(vf1, __builtin_bit_cast(bf16x8, p1), o);                                                            \
    if (PV) { vf0 = wfrag<320>(vq + (VO)); vf1 = wfrag<320>(vq + (VO) + 640); }                                  \
    SBAR();                                                                                                      \
  }
  {
    lds_cptr kq = kp + 2560, kq1 = kp1 + 2560, vq = vp + 1280;
    for (int t = 0; t < 62; t += 2) {
      PIN(vq); PIN(kq); PIN(kq1);
      STEP(sA, sB, 0, 0, true, true, true);
      STEP(sB, sA, 1280, 1280, true, true, true);
      kq += 2560; kq1 += 2560; vq += 2560;
    }
    STEP(sA, sB, 0, 0, true, false, true);
    STEP(sB, sA, 0, 0, false, false, false);
  }
  STAMP(3, 2);
  float l = __shfl(o[10], r32);
  if (__any(!(l < 1.0e30f))) {
    float mrun = -INFINITY;
    o = (f32x16){};
    for (int t = 0; t < 64; ++t) {
      LOADK(t, kf0, kf1);
      LOADV(t, vf0, vf1);
      f32x16 s = MFMA(kf0, q0, (f32x16){});
      s = MFMA(kf1, q1, s);
      float mx = s[0];
#pragma unroll
      for (int r = 1; r < 16; ++r) mx = fmaxf(mx, s[r]);
      mx = fmaxf(mx, __shfl_xor(mx, 32));
      const float mnew = fmaxf(mrun, mx);
      const float alpha = __builtin_amdgcn_exp2f(mrun - mnew);
      mrun = mnew;
#pragma unroll
      for (int r = 0; r < 16; ++r) o[r] *= alpha;
      u32x4 p0, p1;
#pragma unroll
      for (int j = 0; j < 4; ++j) {
        p0[j] = pack2(__builtin_amdgcn_exp2f(s[2 * j] - mnew), __builtin_amdgcn_exp2f(s[2 * j + 1] - mnew));
        p1[j] = pack2(__builtin_amdgcn_exp2f(s[8 + 2 * j] - mnew), __builtin_amdgcn_exp2f(s[9 + 2 * j] - mnew));
      }
      o = MFMA(vf0, __builtin_bit_cast(bf16x8, p0), o);
      o = MFMA(vf1, __builtin_bit_cast(bf16x8, p1), o);
    }
    l = __shfl(o[10], r32);
  }
  const float rl = 1.0f / l;
  char* ob = ws + WS_O + ((size_t)(q >> 5) * 36 * 64 + (q & 31)) * 16;
#pragma unroll
  for (int i = 0; i < 5; ++i) {
    const int d = (i >> 1) * 8 + 4 * hi + (i & 1) * 2;
    const int r = (i >> 1) * 4 + (i & 1) * 2;
    const int f = head * DH_ + d;
    if (i < 4 || !hi) *(unsigned*)(ob + ((f >> 4) * 64 + ((f >> 3) & 1) * 32) * 16 + (f & 7) * 2) = pack2(o[r] * rl, o[r + 1] * rl);
  }
  STAMP(3, 20);
}

extern "C" void kernel_launch(void* const* d_in, const int* in_sizes, int n_in, void* d_out, int out_size,
                              void* d_ws, size_t ws_size, hipStream_t stream) {
  const u16* x = (const u16*)d_in[0];
  const u16* wqkv = (const u16*)d_in[1];
  const u16* wo = (const u16*)d_in[2];
  const u16* w1 = (const u16*)d_in[4];
  const u16* w3 = (const u16*)d_in[5];
  const u16* w2 = (const u16*)d_in[6];
  char* ws = (char*)d_ws;
  u16* out = (u16*)d_out;
  (void)hipFuncSetAttribute((const void*)k_gemm1, hipFuncAttributeMaxDynamicSharedMemorySize, GEMM_LDS);
  (void)hipFuncSetAttribute((const void*)k_down, hipFuncAttributeMaxDynamicSharedMemorySize, GEMM_LDS);
  (void)hipFuncSetAttribute((const void*)k_final, hipFuncAttributeMaxDynamicSharedMemorySize, GEMM_LDS);
  (void)hipFuncSetAttribute((const void*)k_attn, hipFuncAttributeMaxDynamicSharedMemorySize, ATTN_LDS);
  k_ln<<<256, 64, 0, stream>>>(x, ws);
  k_gemm1<<<256, 512, GEMM_LDS, stream>>>(wqkv, w1, w3, ws, (const char*)w2, (const char*)wo);
  k_down<<<256, 512, GEMM_LDS, stream>>>(w2, ws);
  k_attn<<<256, 768, ATTN_LDS, stream>>>(ws);
  k_final<<<288, 128, 2 * SLOT_B + 16, stream>>>(wo, ws, x, out);
}
```

```cpp
#include <hip/hip_runtime.h>
#include <hip/hip_bf16.h>
#include <stdint.h>

typedef unsigned short u16;
typedef short bf16x8 __attribute__((ext_vector_type(8)));
typedef short s16x4 __attribute__((ext_vector_type(4)));
typedef float f32x16 __attribute__((ext_vector_type(16)));
typedef float f32x4 __attribute__((ext_vector_type(4)));
typedef float f32x2 __attribute__((ext_vector_type(2)));
typedef unsigned u32x4 __attribute__((ext_vector_type(4)));
typedef unsigned u32x2 __attribute__((ext_vector_type(2)));
typedef __bf16 bf16x2_t __attribute__((ext_vector_type(2)));
typedef short v4i16_t __attribute__((ext_vector_type(4)));
typedef __attribute__((address_space(3))) const char* lds_cptr;
typedef __attribute__((address_space(3))) char* lds_ptr;

#define DI __device__ __forceinline__
#define MFMA(a, b, c) __builtin_amdgcn_mfma_f32_32x32x16_bf16((a), (b), (c), 0, 0, 0)
#define SBAR() __builtin_amdgcn_sched_barrier(0)

constexpr int S_ = 2048, D_ = 576, H_ = 32, DH_ = 18, F_ = 4608;
constexpr size_t WS_XN = 0;
constexpr size_t WS_Q = WS_XN + (size_t)S_ * D_ * 2;
constexpr size_t QH = 81920;
constexpr size_t WS_KV = WS_Q + (size_t)H_ * QH;
constexpr size_t WS_H = WS_KV + (size_t)H_ * 2 * QH;
constexpr size_t WS_O = WS_H + (size_t)S_ * F_ * 2;
constexpr size_t WS_P = WS_O + (size_t)S_ * D_ * 2;
constexpr size_t WS_PS = WS_P + (size_t)8 * S_ * D_ * 4;
constexpr size_t WS_END = WS_PS + (size_t)S_ * D_ * 4;

DI unsigned pack2(float a, float b) {
  bf16x2_t r = __builtin_convertvector((f32x2){a, b}, bf16x2_t);
  return __builtin_bit_cast(unsigned, r);
}
DI void st16_wt(char* ws, size_t off, u32x4 v) {
  __builtin_amdgcn_raw_buffer_store_b128(v, __builtin_amdgcn_make_buffer_rsrc(ws, 0, 0x7fffffff, 0x00020000), (int)off, 0, 17);
}
DI float bflo(unsigned w) { return __uint_as_float(w << 16); }
DI float bfhi(unsigned w) { return __uint_as_float(w & 0xffff0000u); }

DI void glds16(const void* g, unsigned lds_base) {
  unsigned sv;
  asm volatile("s_mov_b32 %0, m0\n\ts_mov_b32 m0, %2\n\ts_nop 0\n\tglobal_load_lds_dwordx4 %1, off\n\ts_mov_b32 m0, %0"
               : "=&s"(sv) : "v"(g), "s"(lds_base) : "memory");
}
DI s16x4 vtr(lds_cptr p) {
  return __builtin_bit_cast(s16x4, __builtin_amdgcn_ds_read_tr16_b64_v4i16((__attribute__((address_space(3))) v4i16_t*)p));
}
template <int OFF2>
DI bf16x8 wfrag(lds_cptr p) {
  s16x4 lo = vtr(p), hi = vtr(p + OFF2);
  return (bf16x8){lo[0], lo[1], lo[2], lo[3], hi[0], hi[1], hi[2], hi[3]};
}
#define WAITV_BAR(N) asm volatile("s_waitcnt vmcnt(" #N ")\n\ts_barrier" ::: "memory")
#define WAITV(N) asm volatile("s_waitcnt vmcnt(" #N ")" ::: "memory")
#define WAITVN(N) asm volatile("s_waitcnt vmcnt(%0)" :: "i"(N) : "memory")
#define WAIT_BAR(N) asm volatile("s_waitcnt vmcnt(" #N ") lgkmcnt(0)\n\ts_barrier" ::: "memory")

__global__ __launch_bounds__(64) void k_ln(const u16* __restrict__ x, char* __restrict__ ws) {
  const int lane = threadIdx.x & 63, wid = threadIdx.x >> 6;
  const int row = (blockIdx.x + wid) * 8 + (lane >> 3), c8 = lane & 7;
  const u16* xr = x + (size_t)row * D_ + c8 * 8;
  u32x4 a[9];
#pragma unroll
  for (int j = 0; j < 9; ++j) a[j] = *(const u32x4*)(xr + 64 * j);
  float s = 0.f;
#pragma unroll
  for (int j = 0; j < 9; ++j)
#pragma unroll
    for (int i = 0; i < 4; ++i) s += bflo(a[j][i]) + bfhi(a[j][i]);
  s += __shfl_xor(s, 1); s += __shfl_xor(s, 2); s += __shfl_xor(s, 4);
  const float mean = s * (1.0f / D_);
  float q = 0.f;
#pragma unroll
  for (int j = 0; j < 9; ++j)
#pragma unroll
    for (int i = 0; i < 4; ++i) { float d0 = bflo(a[j][i]) - mean, d1 = bfhi(a[j][i]) - mean; q += d0 * d0 + d1 * d1; }
  q += __shfl_xor(q, 1); q += __shfl_xor(q, 2); q += __shfl_xor(q, 4);
  const float mb = bflo(pack2(mean, mean));
  const float vb = bflo(pack2(q * (1.0f / D_), 0.f));
  const float sd = bflo(pack2(sqrtf(bflo(pack2(vb + 1e-5f, 0.f))), 0.f));
  char* xn = ws + WS_XN + ((size_t)(row >> 5) * 36 * 64 + (row & 31)) * 16;
#pragma unroll
  for (int j = 0; j < 9; ++j) {
    u32x4 o;
#pragma unroll
    for (int i = 0; i < 4; ++i) {
      const unsigned dd = pack2(bflo(a[j][i]) - mb, bfhi(a[j][i]) - mb);
      o[i] = pack2(bflo(dd) / sd, bfhi(dd) / sd);
    }
    const int c = c8 + 8 * j;
    *(u32x4*)(xn + ((c >> 1) * 64 + (c & 1) * 32) * 16) = o;
  }
}

constexpr int NSLOT = 4, SLOT_B = 36864;
constexpr int GEMM_LDS = NSLOT * SLOT_B + 16 + 8192;

template <int V> struct IntC { static constexpr int value = V; };
#define PIN(x) asm volatile("" : "+v"(x))

template <int MODE, int NW>
DI void gemm_core(const char* __restrict__ Af  ,
                  int ngu, int gu0, int ndt, int dt0,
                  const u16* __restrict__ W0, const u16* __restrict__ W1, const u16* __restrict__ W2,
                  char* __restrict__ ws, const u16* __restrict__ xin, u16* __restrict__ out,
                  int rowblk  , int kslice, char* lds, int cnt_off, int tail) {
  const int tid = threadIdx.x, lane = tid & 63, r32 = lane & 31, hi = lane >> 5;
  const int wid = __builtin_amdgcn_readfirstlane(tid >> 6);
  const unsigned lds0 = (unsigned)(uintptr_t)lds;
  const int nchunk = 4 * ngu + 2 * ndt + (tail >= 0 ? 2 : 0);
  typedef __attribute__((address_space(3))) volatile unsigned* lds_vu;
  const lds_vu cnt = (lds_vu)((lds_ptr)lds + cnt_off);
  if (tid == 0) *cnt = 0;

  auto chunk_src = [&](int c, int& ld) __attribute__((always_inline)) -> const u16* {
    const u16* base;
    if (MODE == 0) {
      if (c < 4 * ngu) { base = ((c & 2) ? W2 : W1) + 64 * (gu0 + (c >> 2)); ld = F_; }
      else { base = W0 + 64 * (dt0 + ((c - 4 * ngu) >> 1)); ld = 3 * D_; }
    } else if (MODE == 1) {
      base = W0 + (size_t)kslice * 576 * D_ + 64 * ((c >> 1) < ndt ? dt0 + (c >> 1) : (tail >> 1)); ld = D_;
    } else {
      base = W0 + 64 * (dt0 + (c >> 1)); ld = D_;
    }
    const int row = wid * 8 + (lane >> 3);
    return base + (size_t)((c & 1) * 288 + row) * ld + ((lane & 7) ^ (((lane >> 4) & 1) << 2)) * 8;
  };
  constexpr int NPIECE = (36 + NW - 1) / NW;
  auto issue = [&](int c) __attribute__((always_inline)) {
    int ld; const u16* src = chunk_src(c, ld);
    const unsigned dst = lds0 + (c % NSLOT) * SLOT_B + wid * 1024;
#pragma unroll
    for (int i = 0; i < NPIECE; ++i)
      if (i < 36 / NW || wid < 36 % NW)
        glds16(src + (size_t)i * NW * 8 * ld, (unsigned)__builtin_amdgcn_readfirstlane(dst + i * NW * 1024));
  };
  u32x2 pfx[8]; f32x4 pfm[8];
  if (MODE == 2) {
#pragma unroll
    for (int i = 0; i < 8; ++i) {
      const int t = 2 * dt0 + (i >> 2), g = i & 3;
      pfx[i] = *(const u32x2*)(xin + (size_t)(rowblk * 32 + r32) * D_ + 32 * t + 8 * g + 4 * hi);
      pfm[i] = *(const f32x4*)(ws + WS_PS + ((((size_t)rowblk * 18 + t) * 4 + g) * 64 + lane) * 16);
    }
  }
  issue(0);
  issue(1);
  bf16x8 A[36];
  const char* ap = Af + lane * 16;
#define ALOAD(k) asm volatile("global_load_dwordx4 %0, %1, off offset:%2" : "=v"(A[k]) : "v"(ap + ((k) >> 2) * 4096), "i"(((k) & 3) * 1024) : "memory")
#pragma unroll
  for (int s = 0; s < 6; ++s) ALOAD(s);
  asm volatile("s_waitcnt vmcnt(%0) lgkmcnt(0)\n\ts_barrier" :: "i"(6 + 36 / NW) : "memory");
  SBAR();

  constexpr int OFF2 = (MODE == 1) ? 1024 : 512;
  const int q4 = (lane & 15) >> 2;
  const int rdo = ((MODE == 1 ? hi * 4 : hi * 8) + q4) * 128 + ((q4 >> 1) << 6) + ((lane >> 4) & 1) * 32 + (lane & 3) * 8;
  const lds_cptr rdA = (lds_cptr)lds + rdo, rdB = (lds_cptr)lds + (rdo ^ 64);
  const int dB = (rdo ^ 64) - rdo;
  const int m = rowblk * 32 + r32;

  auto epi_h = [&](const u32x4& sa, const u32x4& sb, const f32x16& up, int t32) __attribute__((always_inline)) {
    const size_t off = WS_H + (((size_t)rowblk * 288 + 2 * t32) * 64 + lane) * 16;
    u32x4 w0, w1;
#pragma unroll
    for (int j = 0; j < 4; ++j) {
      w0[j] = pack2(bflo(sa[j]) * up[2 * j], bfhi(sa[j]) * up[2 * j + 1]);
      w1[j] = pack2(bflo(sb[j]) * up[8 + 2 * j], bfhi(sb[j]) * up[9 + 2 * j]);
    }
    st16_wt(ws, off, w0);
    st16_wt(ws, off + 1024, w1);
  };
  auto epi_single = [&](const f32x16& a, int t) __attribute__((always_inline)) {
    if (MODE == 0) {
      const lds_ptr stg = (lds_ptr)lds + NSLOT * SLOT_B + 16 + wid * 1024;
      const int n = 32 * t + 2 * (lane & 15);
      const int which = n / D_, f = n - which * D_, head = f / DH_, d = f - head * DH_;
      const size_t off = (which == 0 ? (WS_Q + (size_t)head * QH) : (WS_KV + (size_t)head * 2 * QH + (size_t)(which - 1) * QH)) +
                         (size_t)(rowblk * 32 + (lane >> 4)) * 40 + d * 2;
      const float sc = (32 * t < D_) ? (0.23570226039551584f * 1.4426950408889634f) : 1.0f;
#pragma unroll
      for (int rho = 0; rho < 2; ++rho) {
        if ((r32 >> 4) == rho) {
#pragma unroll
          for (int g = 0; g < 4; ++g)
            *(__attribute__((address_space(3))) u32x2*)(stg + (r32 & 15) * 64 + (8 * g + 4 * hi) * 2) =
                (u32x2){pack2(a[4 * g] * sc, a[4 * g + 1] * sc), pack2(a[4 * g + 2] * sc, a[4 * g + 3] * sc)};
        }
        asm volatile("s_waitcnt lgkmcnt(0)" ::: "memory");
        unsigned wv[4];
#pragma unroll
        for (int i = 0; i < 4; ++i) wv[i] = *(__attribute__((address_space(3))) const unsigned*)(stg + ((lane >> 4) + 4 * i) * 64 + (lane & 15) * 4);
#pragma unroll
        for (int i = 0; i < 4; ++i) *(unsigned*)(ws + off + (size_t)(16 * rho + 4 * i) * 40) = wv[i];
        asm volatile("s_waitcnt lgkmcnt(0)" ::: "memory");
      }
    } else if (MODE == 1) {
      const size_t off = WS_P + (((((size_t)kslice * 64 + rowblk) * 18 + t) * 2) * 64 + lane) * 16;
#pragma unroll
      for (int j = 0; j < 2; ++j)
        st16_wt(ws, off + j * 1024, (u32x4){pack2(a[8 * j], a[8 * j + 1]), pack2(a[8 * j + 2], a[8 * j + 3]),
                                            pack2(a[8 * j + 4], a[8 * j + 5]), pack2(a[8 * j + 6], a[8 * j + 7])});
    } else {
#pragma unroll
      for (int g = 0; g < 4; ++g) {
        const int col = 32 * t + 8 * g + 4 * hi;
        const u32x2 xv = pfx[(t & 1) * 4 + g];
        const f32x4 pm = pfm[(t & 1) * 4 + g];
        f32x4 sum = {a[4 * g] + pm[0] + bflo(xv[0]), a[4 * g + 1] + pm[1] + bfhi(xv[0]),
                     a[4 * g + 2] + pm[2] + bflo(xv[1]), a[4 * g + 3] + pm[3] + bfhi(xv[1])};
        *(u32x2*)(out + (size_t)m * D_ + col) = (u32x2){pack2(sum[0], sum[1]), pack2(sum[2], sum[3])};
      }
    }
  };

  bf16x8 bq[6];
#pragma unroll
  for (int s = 0; s < 5; ++s) bq[s] = wfrag<OFF2>(rdA + s * 2048);

  int c = 0;
  auto run_chunk = [&](f32x16& aX, f32x16& aY, auto half_, auto sil_, auto nst_, auto start_, f32x16& sg, u32x4& s0, u32x4& s1, int nrt) __attribute__((always_inline)) {
    constexpr int HALF = decltype(half_)::value, SIL = decltype(sil_)::value, NST = decltype(nst_)::value, START = decltype(start_)::value;
    int so = __builtin_amdgcn_readfirstlane((c % NSLOT) * SLOT_B);
    const int cn = c + 1 < nchunk ? c + 1 : c;
    int sn = __builtin_amdgcn_readfirstlane((cn % NSLOT) * SLOT_B);
    asm volatile("" : "+s"(so), "+s"(sn));
    const lds_cptr pn = rdA + sn + ((MODE == 1 && tail >= 0 && c + 3 == nchunk && (tail & 1)) ? dB : 0);
    const bool doiss = START != 1 && c + 3 < nchunk;
    int nld; const u16* nsrc = chunk_src(doiss ? c + 3 : c, nld);
    const size_t nstr = (size_t)NW * 8 * nld;
    const unsigned ndst = (unsigned)__builtin_amdgcn_readfirstlane(lds0 + ((c + 3) % NSLOT) * SLOT_B + wid * 1024);
    const unsigned target = (unsigned)(NW * (c + 1));
    unsigned fl = 0;
    float sv[2], e1[2], e2[2];
#pragma unroll
    for (int s = 0; s < 36; ++s) {
      if (NW == 8 && s == 0) __builtin_amdgcn_s_setprio(2);
      if (NW == 8 && s == 18) __builtin_amdgcn_s_setprio(0);
      if (NW == 8 && s == (START == 2 ? 20 : 1)) {
        PIN(aX); PIN(aY);
        if (START == 2) {
          if (c + 2 < nchunk) { WAITV(4); } else { WAITV(0); }
        } else if (START == 0) {
          if (c + 2 < nchunk) {
            if (NST >= 0) { if (wid < 4) { WAITVN(5 + (NST >= 0 ? NST : 0)); } else { WAITVN(4 + (NST >= 0 ? NST : 0)); } }
            else if (nrt == 16) { if (wid < 4) { WAITVN(21); } else { WAITVN(20); } }
            else { if (wid < 4) { WAITVN(9); } else { WAITVN(8); } }
          } else { WAITV(0); }
        }
        if (lane == 0) __hip_atomic_fetch_add((__attribute__((address_space(3))) unsigned*)((lds_ptr)lds + cnt_off), 1u, __ATOMIC_RELAXED, __HIP_MEMORY_SCOPE_WORKGROUP);
      }
      if (NW != 8 && START == 1 && s == 26) { PIN(aX); PIN(aY); asm volatile("s_barrier" ::: "memory"); }
      if (NW == 8 && s == 23) fl = *cnt;
      if (NW == 8 && s == 26) {
        PIN(aX); PIN(aY);
        while ((unsigned)__builtin_amdgcn_readfirstlane(fl) < target) { __builtin_amdgcn_s_sleep(1); fl = *cnt; }
        asm volatile("" ::: "memory");
      }
      if (NW == 8 && s >= 27 && (s - 27) % 2 == 0) {
        const int i = (s - 27) / 2;
        PIN(aY);
        if (doiss && (i < 4 || wid < 4)) glds16(nsrc + i * nstr, ndst + i * NW * 1024);
      }
      {
        const int sp = s + 5;
        if (sp < 18) bq[sp % 6] = wfrag<OFF2>(rdA + so + sp * 2048);
        else if (sp < 36) bq[sp % 6] = wfrag<OFF2>(rdB + so + (sp - 18) * 2048);
        else bq[sp % 6] = wfrag<OFF2>(pn + (sp - 36) * 2048);
      }
      if (START == 1 && s < 18) asm volatile("s_waitcnt vmcnt(%1)" : "+v"(A[s]) : "i"(s <= 15 ? 5 + s : 35 - s));
      if (START == 2 && s < 18) asm volatile("s_waitcnt vmcnt(%1)" : "+v"(A[18 + s]) : "i"(17 - s + (NW == 8 ? 8 : 0)));
      if (s < 18) {
        if (HALF == 0 && s == 0) aX = MFMA(bq[0], A[0], (f32x16){});
        else aX = MFMA(bq[s % 6], A[18 * HALF + s], aX);
      } else {
        if (HALF == 0 && s == 18) aY = MFMA(bq[s % 6], A[0], (f32x16){});
        else aY = MFMA(bq[s % 6], A[18 * HALF + s - 18], aY);
      }
      if (START == 1) {
        if (s <= 14) { ALOAD(6 + 2 * s); ALOAD(7 + 2 * s); }
        if (NW == 8 && s >= 18 && s < 28) {
          const int cc = 2 + (s - 18) / 5, i = (s - 18) % 5;
          int ld2; const u16* src2 = chunk_src(cc, ld2);
          if (i < 4 || wid < 4) glds16(src2 + (size_t)i * NW * 8 * ld2, (unsigned)__builtin_amdgcn_readfirstlane(lds0 + cc * SLOT_B + wid * 1024 + i * NW * 1024));
        }
      }
      if (SIL != 0) {
        constexpr int B0 = (SIL == 1) ? 18 : 0;
        const f32x16& g = (SIL == 1) ? aX : sg;
        const int r1 = s - B0, r2 = s - B0 - 1, r3 = s - B0 - 2;
        if (r3 >= 0 && r3 < 16) {
          sv[r3 & 1] = g[r3] * e2[r3 & 1];
          if (r3 & 1) { unsigned w = pack2(sv[0], sv[1]); PIN(w); if (r3 < 8) s0[r3 >> 1] = w; else s1[(r3 - 8) >> 1] = w; }
        }
        if (r2 >= 0 && r2 < 16) { e2[r2 & 1] = __builtin_amdgcn_rcpf(1.0f + e1[r2 & 1]); PIN(e2[r2 & 1]); }
        if (r1 >= 0 && r1 < 16) { e1[r1 & 1] = __builtin_amdgcn_exp2f(-1.4426950408889634f * g[r1]); PIN(e1[r1 & 1]); }
      }
      SBAR();
    }
    ++c;
  };

  auto run_tail = [&](f32x16& acc) __attribute__((always_inline)) {
    int so = __builtin_amdgcn_readfirstlane((c % NSLOT) * SLOT_B), sn = __builtin_amdgcn_readfirstlane(((c + 1) % NSLOT) * SLOT_B);
    asm volatile("" : "+s"(so), "+s"(sn));
    const lds_cptr rdO = rdA + ((tail & 1) ? dB : 0);
    const unsigned target = (unsigned)(NW * (c + 1));
    unsigned fl = 0;
#pragma unroll
    for (int s = 0; s < 36; ++s) {
      if (s == 1) {
        PIN(acc);
        WAITV(0);
        if (lane == 0) __hip_atomic_fetch_add((__attribute__((address_space(3))) unsigned*)((lds_ptr)lds + cnt_off), 1u, __ATOMIC_RELAXED, __HIP_MEMORY_SCOPE_WORKGROUP);
      }
      if (s == 9) fl = *cnt;
      if (s == 12) {
        PIN(acc);
        while ((unsigned)__builtin_amdgcn_readfirstlane(fl) < target) { __builtin_amdgcn_s_sleep(1); fl = *cnt; }
        asm volatile("" ::: "memory");
      }
      const int sp = s + 5;
      if (sp < 18) bq[sp % 6] = wfrag<OFF2>(rdO + so + sp * 2048);
      else if (sp < 36) bq[sp % 6] = wfrag<OFF2>(rdO + sn + (sp - 18) * 2048);
      if (s == 0) acc = MFMA(bq[0], A[0], (f32x16){});
      else acc = MFMA(bq[s % 6], A[s], acc);
      SBAR();
    }
  };

  f32x16 a0, a1;
  u32x4 sa0, sa1, sb0, sb1;
  typedef IntC<0> I0; typedef IntC<1> I1; typedef IntC<2> I2;
  if (MODE == 0) {
    run_chunk(a0, a1, I0{}, I0{}, I0{}, I1{}, a1, sa0, sa1, 0);
    run_chunk(a0, a1, I1{}, I1{}, I0{}, I2{}, a1, sa0, sa1, 0);
    for (int gi = 0; gi < ngu; ++gi) {
      run_chunk(a0, a1, I0{}, I2{}, I0{}, I0{}, a1, sb0, sb1, 0);
      run_chunk(a0, a1, I1{}, I0{}, I0{}, I0{}, a1, sb0, sb1, 0);
      epi_h(sa0, sa1, a0, 2 * (gu0 + gi));
      epi_h(sb0, sb1, a1, 2 * (gu0 + gi) + 1);
      if (gi + 1 < ngu) {
        run_chunk(a0, a1, I0{}, I0{}, IntC<4>{}, I0{}, a1, sa0, sa1, 0);
        run_chunk(a0, a1, I1{}, I1{}, I0{}, I0{}, a1, sa0, sa1, 0);
      }
    }
    for (int di = 0; di < ndt; ++di) {
      run_chunk(a0, a1, I0{}, I0{}, IntC<-1>{}, I0{}, a1, sa0, sa1, di ? 16 : 4);
      run_chunk(a0, a1, I1{}, I0{}, I0{}, I0{}, a1, sa0, sa1, 0);
      epi_single(a0, 2 * (dt0 + di));
      epi_single(a1, 2 * (dt0 + di) + 1);
    }
  } else {
    run_chunk(a0, a1, I0{}, I0{}, I0{}, I1{}, a1, sa0, sa1, 0);
    run_chunk(a0, a1, I1{}, I0{}, I0{}, I2{}, a1, sa0, sa1, 0);
    epi_single(a0, 2 * dt0);
    epi_single(a1, 2 * dt0 + 1);
    for (int di = 1; di < ndt; ++di) {
      run_chunk(a0, a1, I0{}, I0{}, IntC<4>{}, I0{}, a1, sa0, sa1, 0);
      run_chunk(a0, a1, I1{}, I0{}, I0{}, I0{}, a1, sa0, sa1, 0);
      epi_single(a0, 2 * (dt0 + di));
      epi_single(a1, 2 * (dt0 + di) + 1);
    }
    if (MODE == 1 && tail >= 0) {
      run_tail(a0);
      epi_single(a0, tail);
    }
  }
}

__global__ __launch_bounds__(512, 2) void k_gemm1(const u16* __restrict__ wqkv, const u16* __restrict__ w1,
                                                  const u16* __restrict__ w3, char* __restrict__ ws,
                                                  const char* __restrict__ w2b, const char* __restrict__ wob) {
  extern __shared__ __attribute__((aligned(16))) char lds[];
  const int b = blockIdx.x, xcd = b & 7, idx = b >> 3;
  const int panel = idx & 7, cg = xcd * 4 + (idx >> 3);
  int ngu, gu0, ndt, dt0;
  if (cg < 11) { ngu = 3; gu0 = 3 * cg; ndt = 0; dt0 = 0; }
  else if (cg < 29) { const int j = cg - 11; ngu = 2; gu0 = 33 + 2 * j; ndt = 1; dt0 = j; }
  else { const int j = cg - 29; ngu = 1; gu0 = 69 + j; ndt = 3; dt0 = 18 + 3 * j; }
  const int rowblk = panel * 8 + (threadIdx.x >> 6);
  gemm_core<0, 8>(ws + WS_XN + (size_t)rowblk * 36 * 1024, ngu, gu0, ndt, dt0, wqkv, w1, w3, ws, nullptr, nullptr,
                  rowblk, 0, lds, NSLOT * SLOT_B, -1);
  if (cg >= 11 && cg < 29) {
    const int w = ((cg - 11) * 8 + panel) * 8 + (threadIdx.x >> 6);
    const int lane = threadIdx.x & 63;
    u32x4 acc = {0, 0, 0, 0};
#pragma unroll
    for (int i = 0; i < 6; ++i) {
      const int pc = w + 1152 * i;
      if (pc < 5184) acc ^= *(const u32x4*)(w2b + (size_t)pc * 1024 + lane * 16);
      else if (pc < 5832) acc ^= *(const u32x4*)(wob + (size_t)(pc - 5184) * 1024 + lane * 16);
    }
    asm volatile("" :: "v"(acc));
  }
}

__global__ __launch_bounds__(512, 2) void k_down(const u16* __restrict__ w2, char* __restrict__ ws) {
  extern __shared__ __attribute__((aligned(16))) char lds[];
  const int b = blockIdx.x, ks = b & 7, idx = b >> 3;
  const int panel = idx & 7, nq = idx >> 3;
  const int ndt = 2, dt0 = 2 * nq, tail = nq < 2 ? 16 + nq : -1;
  const int rowblk = panel * 8 + (threadIdx.x >> 6);
  gemm_core<1, 8>(ws + WS_H + ((size_t)rowblk * 288 + ks * 36) * 1024, 0, 0, ndt, dt0, w2, nullptr, nullptr, ws, nullptr,
                  nullptr, rowblk, ks, lds, NSLOT * SLOT_B, tail);
}

__global__ __launch_bounds__(256, 2) void k_final(const u16* __restrict__ wo, char* __restrict__ ws,
                                                  const u16* __restrict__ x, u16* __restrict__ out) {
  extern __shared__ __attribute__((aligned(16))) char lds[];
  const int b = blockIdx.x, rp = b & 15, u = b >> 4;
  const int rowblk = rp * 4 + (threadIdx.x >> 6);
  gemm_core<2, 4>(ws + WS_O + (size_t)rowblk * 36 * 1024, 0, 0, 1, u, wo, nullptr, nullptr, ws, x, out, rowblk, 0, lds, 2 * SLOT_B, -1);
}

constexpr int ATTN_LDS = 163840;

__global__ __launch_bounds__(768, 3) void k_attn(char* __restrict__ ws) {
  extern __shared__ __attribute__((aligned(16))) char lds[];
  const int tid = threadIdx.x, lane = tid & 63, r32 = lane & 31, hi = lane >> 5;
  const int wid = __builtin_amdgcn_readfirstlane(tid >> 6);
  const int b = blockIdx.x, xcd = b & 7, idx = b >> 3;
  const int head = xcd * 4 + (idx >> 3), qb = idx & 7;
  const unsigned lds0 = (unsigned)(uintptr_t)lds;
  if (wid >= 8) {
    WAITV_BAR(0);
    WAIT_BAR(0);
    for (int pc = wid - 8; pc < 9; pc += 4) {
      const size_t pidx = (size_t)b * 9 + pc;
      u32x4 v[8];
#pragma unroll
      for (int sl = 0; sl < 8; ++sl) v[sl] = __builtin_nontemporal_load((const u32x4*)(ws + WS_P + (size_t)sl * (64 * 18 * 2 * 1024) + pidx * 1024 + lane * 16));
      f32x4 s0 = {0.f, 0.f, 0.f, 0.f}, s1 = {0.f, 0.f, 0.f, 0.f};
#pragma unroll
      for (int sl = 0; sl < 8; ++sl) {
        s0 += (f32x4){bflo(v[sl][0]), bfhi(v[sl][0]), bflo(v[sl][1]), bfhi(v[sl][1])};
        s1 += (f32x4){bflo(v[sl][2]), bfhi(v[sl][2]), bflo(v[sl][3]), bfhi(v[sl][3])};
      }
      *(f32x4*)(ws + WS_PS + pidx * 2048 + lane * 16) = s0;
      *(f32x4*)(ws + WS_PS + pidx * 2048 + 1024 + lane * 16) = s1;
    }
    return;
  }
  {
    const char* src = ws + WS_KV + (size_t)head * 2 * QH + wid * 1024 + lane * 16;
#pragma unroll
    for (int i = 0; i < 20; ++i) glds16(src + i * 8192, (unsigned)__builtin_amdgcn_readfirstlane(lds0 + wid * 1024 + i * 8192));
  }
  const int q = qb * 256 + wid * 32 + r32;
  const char* qp = ws + WS_Q + (size_t)head * QH + (size_t)q * 40;
  u32x2 qa = *(const u32x2*)(qp + 16 * hi), qb2 = *(const u32x2*)(qp + 16 * hi + 8);
  unsigned q1w = *(const unsigned*)(qp + 32);
  asm volatile("" : "+v"(qa), "+v"(qb2), "+v"(q1w));
  WAITV_BAR(0);
  if (hi) q1w = 0;
  const bf16x8 q0 = __builtin_bit_cast(bf16x8, (u32x4){qa[0], qa[1], qb2[0], qb2[1]});
  const bf16x8 q1 = __builtin_bit_cast(bf16x8, (u32x4){q1w, 0u, 0u, 0u});
#pragma unroll
  for (int i = 0; i < 4; ++i) *(__attribute__((address_space(3))) unsigned*)((lds_ptr)lds + QH + (size_t)(tid + 512 * i) * 40 + 36) = 0x00003f80u;
  WAIT_BAR(0);

  typedef __attribute__((address_space(3))) const u32x2* lds_u2;
  typedef __attribute__((address_space(3))) const unsigned* lds_u;
  const lds_cptr kp = (lds_cptr)lds + r32 * 40 + 16 * hi;
  const lds_cptr kp1 = (lds_cptr)lds + r32 * 40 + 32;
  const lds_cptr vp = (lds_cptr)lds + QH + (4 * hi + ((lane & 15) >> 2)) * 40 + (((lane >> 4) & 1) ? 32 : (lane & 3) * 8);

#define LOADK(t, kf0, kf1)                                                                   \
  {                                                                                          \
    u32x2 a_ = *(lds_u2)(kp + (t) * 1280), b_ = *(lds_u2)(kp + (t) * 1280 + 8);              \
    kf0 = __builtin_bit_cast(bf16x8, (u32x4){a_[0], a_[1], b_[0], b_[1]});                   \
    kf1 = __builtin_bit_cast(bf16x8, (u32x4){*(lds_u)(kp1 + (t) * 1280), 0u, 0u, 0u});       \
  }
#define LOADV(t, vf0, vf1)                                                                   \
  {                                                                                          \
    vf0 = wfrag<320>(vp + (t) * 1280);                                                       \
    vf1 = wfrag<320>(vp + (t) * 1280 + 640);                                                 \
  }
#define EX(x) __builtin_amdgcn_exp2f(x)

  f32x16 negm;
  bf16x8 kf0, kf1, vf0, vf1;
  {
    LOADK(0, kf0, kf1);
    f32x16 s = MFMA(kf0, q0, (f32x16){});
    s = MFMA(kf1, q1, s);
    float mx = s[0];
#pragma unroll
    for (int r = 1; r < 16; ++r) mx = fmaxf(mx, s[r]);
    mx = fmaxf(mx, __shfl_xor(mx, 32));
#pragma unroll
    for (int r = 0; r < 16; ++r) negm[r] = -mx;
  }
  f32x16 o = {};
  f32x16 sA, sB;
  sA = MFMA(kf0, q0, negm);
  sA = MFMA(kf1, q1, sA);
  LOADK(1, kf0, kf1);
  LOADV(0, vf0, vf1);
#define LOADK2(P0, P1, OFF)                                                                  \
  {                                                                                          \
    u32x2 a_ = *(lds_u2)((P0) + (OFF));                                                      \
    asm volatile("" ::: "memory");                                                           \
    u32x2 b_ = *(lds_u2)((P0) + (OFF) + 8);                                                  \
    kf0 = __builtin_bit_cast(bf16x8, (u32x4){a_[0], a_[1], b_[0], b_[1]});                   \
    kf1 = __builtin_bit_cast(bf16x8, (u32x4){*(lds_u)((P1) + (OFF)), 0u, 0u, 0u});           \
  }
#define STEP(SC, SN, KO, VO, NX, PK, PV)                                                                         \
  {                                                                                                              \
    u32x4 p0, p1;                                                                                                \
    if (NX) SN = MFMA(kf0, q0, negm);                                                                            \
    SC[0] = EX(SC[0]); SC[1] = EX(SC[1]); SC[2] = EX(SC[2]); SC[3] = EX(SC[3]);                                  \
    SC[4] = EX(SC[4]); SC[5] = EX(SC[5]);                                                                        \
    p0[0] = pack2(SC[0], SC[1]); p0[1] = pack2(SC[2], SC[3]); PIN(p0); SBAR();                                   \
    if (NX) SN = MFMA(kf1, q1, SN);                                                                              \
    if (PK) LOADK2(kq, kq1, KO);                                                                                 \
    SC[6] = EX(SC[6]); SC[7] = EX(SC[7]); SC[8] = EX(SC[8]); SC[9] = EX(SC[9]);                                  \
    p0[2] = pack2(SC[4], SC[5]); p0[3] = pack2(SC[6], SC[7]); PIN(p0); SBAR();                                   \
    SC[10] = EX(SC[10]); SC[11] = EX(SC[11]);                                                                    \
    o = MFMA(vf0, __builtin_bit_cast(bf16x8, p0), o);                                                            \
    SC[12] = EX(SC[12]); SC[13] = EX(SC[13]);                                                                    \
    p1[0] = pack2(SC[8], SC[9]); p1[1] = pack2(SC[10], SC[11]); PIN(p1); SBAR();                                 \
    SC[14] = EX(SC[14]); SC[15] = EX(SC[15]);                                                                    \
    p1[2] = pack2(SC[12], SC[13]); PIN(p1); SBAR();                                                              \
    p1[3] = pack2(SC[14], SC[15]); PIN(p1); SBAR();                                                              \
    o = MFMA(vf1, __builtin_bit_cast(bf16x8, p1), o);                                                            \
    if (PV) { vf0 = wfrag<320>(vq + (VO)); vf1 = wfrag<320>(vq + (VO) + 640); }                                  \
    SBAR();                                                                                                      \
  }
  {
    lds_cptr kq = kp + 2560, kq1 = kp1 + 2560, vq = vp + 1280;
    for (int t = 0; t < 62; t += 2) {
      PIN(vq); PIN(kq); PIN(kq1);
      STEP(sA, sB, 0, 0, true, true, true);
      STEP(sB, sA, 1280, 1280, true, true, true);
      kq += 2560; kq1 += 2560; vq += 2560;
    }
    STEP(sA, sB, 0, 0, true, false, true);
    STEP(sB, sA, 0, 0, false, false, false);
  }
  float l = __shfl(o[10], r32);
  if (__any(!(l < 1.0e30f))) {
    float mrun = -INFINITY;
    o = (f32x16){};
    for (int t = 0; t < 64; ++t) {
      LOADK(t, kf0, kf1);
      LOADV(t, vf0, vf1);
      f32x16 s = MFMA(kf0, q0, (f32x16){});
      s = MFMA(kf1, q1, s);
      float mx = s[0];
#pragma unroll
      for (int r = 1; r < 16; ++r) mx = fmaxf(mx, s[r]);
      mx = fmaxf(mx, __shfl_xor(mx, 32));
      const float mnew = fmaxf(mrun, mx);
      const float alpha = __builtin_amdgcn_exp2f(mrun - mnew);
      mrun = mnew;
#pragma unroll
      for (int r = 0; r < 16; ++r) o[r] *= alpha;
      u32x4 p0, p1;
#pragma unroll
      for (int j = 0; j < 4; ++j) {
        p0[j] = pack2(__builtin_amdgcn_exp2f(s[2 * j] - mnew), __builtin_amdgcn_exp2f(s[2 * j + 1] - mnew));
        p1[j] = pack2(__builtin_amdgcn_exp2f(s[8 + 2 * j] - mnew), __builtin_amdgcn_exp2f(s[9 + 2 * j] - mnew));
      }
      o = MFMA(vf0, __builtin_bit_cast(bf16x8, p0), o);
      o = MFMA(vf1, __builtin_bit_cast(bf16x8, p1), o);
    }
    l = __shfl(o[10], r32);
  }
  const float rl = 1.0f / l;
  char* ob = ws + WS_O + ((size_t)(q >> 5) * 36 * 64 + (q & 31)) * 16;
#pragma unroll
  for (int i = 0; i < 5; ++i) {
    const int d = (i >> 1) * 8 + 4 * hi + (i & 1) * 2;
    const int r = (i >> 1) * 4 + (i & 1) * 2;
    const int f = head * DH_ + d;
    if (i < 4 || !hi) *(unsigned*)(ob + ((f >> 4) * 64 + ((f >> 3) & 1) * 32) * 16 + (f & 7) * 2) = pack2(o[r] * rl, o[r + 1] * rl);
  }
}

extern "C" void kernel_launch(void* const* d_in, const int* in_sizes, int n_in, void* d_out, int out_size,
                              void* d_ws, size_t ws_size, hipStream_t stream) {
  const u16* x = (const u16*)d_in[0];
  const u16* wqkv = (const u16*)d_in[1];
  const u16* wo = (const u16*)d_in[2];
  const u16* w1 = (const u16*)d_in[4];
  const u16* w3 = (const u16*)d_in[5];
  const u16* w2 = (const u16*)d_in[6];
  char* ws = (char*)d_ws;
  u16* out = (u16*)d_out;
  (void)hipFuncSetAttribute((const void*)k_gemm1, hipFuncAttributeMaxDynamicSharedMemorySize, GEMM_LDS);
  (void)hipFuncSetAttribute((const void*)k_down, hipFuncAttributeMaxDynamicSharedMemorySize, GEMM_LDS);
  (void)hipFuncSetAttribute((const void*)k_final, hipFuncAttributeMaxDynamicSharedMemorySize, GEMM_LDS);
  (void)hipFuncSetAttribute((const void*)k_attn, hipFuncAttributeMaxDynamicSharedMemorySize, ATTN_LDS);
  k_ln<<<256, 64, 0, stream>>>(x, ws);
  k_gemm1<<<256, 512, GEMM_LDS, stream>>>(wqkv, w1, w3, ws, (const char*)w2, (const char*)wo);
  k_down<<<256, 512, GEMM_LDS, stream>>>(w2, ws);
  k_attn<<<256, 768, ATTN_LDS, stream>>>(ws);
  k_final<<<144, 256, 2 * SLOT_B + 16, stream>>>(wo, ws, x, out);
}
```
